```python
import torch

def _softmax(x, axis=-1):
    return torch.softmax(x, dim=axis)
def _gelu(x):
    return torch.nn.functional.gelu(x, approximate="tanh")
def _silu(x):
    return torch.nn.functional.silu(x)
def _sigmoid(x):
    return torch.sigmoid(x)
def _layernorm(x, axis=-1, eps=1e-5):
    m = x.mean(dim=axis, keepdim=True)
    v = x.var(dim=axis, keepdim=True, unbiased=False)
    return (x - m) / torch.sqrt(v + eps)
def _scatter_add(tgt, idx, src, axis=0):
    return tgt.clone().index_add_(axis, idx, src)
def _segment_sum(src, seg, n):
    out = torch.zeros((n,) + src.shape[1:], dtype=src.dtype)
    return out.index_add_(0, seg, src)
def _pad(x, pw):
    flat = tuple(v for pair in reversed(pw) for v in pair)
    return torch.nn.functional.pad(x, flat)

kernel_name = 'standalone__window_attn_swin__vision'

def setup_inputs():

    _w = (lambda a: (lambda t: t.to(torch.bfloat16) if t.is_floating_point() else t)(torch.as_tensor(a)))
    H = W = 384
    C = 192
    ws = 8
    nh = 4
    shift = ws // 2
    coords = torch.stack(torch.meshgrid(torch.arange(ws), torch.arange(ws), indexing='ij'), 0).reshape(2, -1)
    rel = coords[:, :, None] - coords[:, None, :]
    rel[0] += ws - 1
    rel[1] += ws - 1
    rel[0] *= 2 * ws - 1
    rp_idx = rel.sum(0).to(torch.int64)
    img_mask = torch.zeros((1, H, W, 1), dtype=float)
    cnt = 0
    for hs in (slice(0, H - ws), slice(H - ws, H - shift), slice(H - shift, H)):
        for vs in (slice(0, W - ws), slice(W - ws, W - shift), slice(W - shift, W)):
            img_mask[:, hs, vs, :] = cnt
            cnt += 1
    mw = img_mask.reshape(1, H // ws, ws, W // ws, ws, 1).permute(0, 1, 3, 2, 4, 5).reshape(-1, ws * ws)
    am = mw[:, :, None] - mw[:, None, :]
    attn_mask = torch.where(am != 0, -100.0, 0.0).to(float)
    return {
        "x": _w((lambda *s: torch.randn(*s, dtype=torch.bfloat16) * 1.6)(1, H, W, C)),
        "wqkv": _w((lambda *s: torch.randn(*s, dtype=torch.bfloat16) * 1.6)(C, 3 * C) * 0.02),
        "wo": _w((lambda *s: torch.randn(*s, dtype=torch.bfloat16) * 0.05)(C, C) * 0.02),
        "rpb": _w((lambda *s: torch.randn(*s, dtype=torch.bfloat16) * 0.2)((2 * ws - 1) * (2 * ws - 1), nh)),
        "rp_idx": _w(rp_idx),
        "attn_mask": _w(attn_mask),
    }

def reference(x, wqkv, wo, rpb, rp_idx, attn_mask):
    B, H, W, C = x.shape
    N = rp_idx.shape[0]
    ws = int(round(N ** 0.5))
    nh = rpb.shape[-1]
    hd = C // nh
    shift = ws // 2
    xs = torch.roll(x, (-shift, -shift), (1, 2))
    nWh = H // ws
    nWw = W // ws
    xw = (lambda x, *d: x.permute(*d))(xs.reshape(B, nWh, ws, nWw, ws, C), 0, 1, 3, 2, 4, 5).reshape(B * nWh * nWw, N, C)
    qkv = (lambda x, *d: x.permute(*d))(torch.einsum('wnc,cd->wnd', xw, wqkv).reshape(-1, N, 3, nh, hd), 2, 0, 3, 1, 4)
    q, k, v = qkv[0], qkv[1], qkv[2]
    attn = torch.einsum('whid,whjd->whij', q, k) * (hd ** -0.5)
    bias = (lambda x, *d: x.permute(*d))(rpb[rp_idx.reshape(-1)].reshape(N, N, nh), 2, 0, 1)
    attn = attn + bias[None, :, :, :]
    attn = (attn.reshape(B, nWh * nWw, nh, N, N) + attn_mask[None, :, None, :, :]).reshape(-1, nh, N, N)
    attn = _softmax(attn, axis=-1)
    out = (lambda x, *d: x.permute(*d))(torch.einsum('whij,whjd->whid', attn, v), 0, 2, 1, 3).reshape(-1, N, C)
    out = torch.einsum('wnc,cd->wnd', out, wo)
    out = (lambda x, *d: x.permute(*d))(out.reshape(B, nWh, nWw, ws, ws, C), 0, 1, 3, 2, 4, 5).reshape(B, H, W, C)
    return torch.roll(out, (shift, shift), (1, 2))
```

```cpp
#include <hip/hip_runtime.h>
#include <hip/hip_bf16.h>

typedef float    f32x16 __attribute__((ext_vector_type(16)));
typedef float    f32x4  __attribute__((ext_vector_type(4)));
typedef float    f32x2  __attribute__((ext_vector_type(2)));
typedef __bf16   bf16x8 __attribute__((ext_vector_type(8)));
typedef __bf16   bf16x2 __attribute__((ext_vector_type(2)));
typedef unsigned u32x4  __attribute__((ext_vector_type(4)));
#define DI __device__ __forceinline__

constexpr int IMG = 384, CH = 192, ROWB = IMG * CH * 2;
constexpr int NWIN = 2304;

constexpr size_t WS_WQK = 0;
constexpr size_t WS_WV0 = WS_WQK + 4 * 36 * 1024;
constexpr size_t WS_WV1 = WS_WV0 + 4 * 12 * 1024;
constexpr size_t WS_WO  = WS_WV1 + 4 * 6 * 1024;
constexpr size_t WS_BIAS = WS_WO + 72 * 1024;

DI int tau32(int rho) {
  return 16 * ((rho >> 2) & 1) + 4 * (rho >> 3) + (rho & 3);
}
DI unsigned cvtpk(float lo, float hi) {
  f32x2 v = {lo, hi}; bf16x2 b = __builtin_convertvector(v, bf16x2); return __builtin_bit_cast(unsigned, b);
}

__global__ void prep_kernel(const unsigned short* __restrict__ wqkv, const unsigned short* __restrict__ wo,
                            const unsigned short* __restrict__ rpb, const int* __restrict__ rp_idx, char* __restrict__ ws) {
  const int gid = blockIdx.x * blockDim.x + threadIdx.x;
  const int l = gid & 63, f = gid >> 6, r = l & 31, h = l >> 5;
  unsigned short v[8];
  if (f < 144) {
    const int hd = f / 36, dt = (f % 36) / 12, ks = f % 12;
    const int u = 32 * dt + r;
    const int col = (u < 48) ? hd * 48 + u : 192 + hd * 48 + (u - 48);
    for (int j = 0; j < 8; ++j) v[j] = wqkv[(16 * ks + 8 * h + j) * 576 + col];
  } else if (f < 192) {
    const int ff = f - 144, hd = ff / 12, ks = ff % 12;
    for (int j = 0; j < 8; ++j) v[j] = wqkv[(16 * ks + 8 * h + j) * 576 + 384 + hd * 48 + r];
  } else if (f < 216) {
    const int ff = f - 192, hd = ff / 6, ks = ff % 6;
    for (int j = 0; j < 8; ++j) v[j] = wqkv[(32 * ks + 8 * (l >> 4) + j) * 576 + 384 + hd * 48 + 32 + (l & 15)];
  } else if (f < 288) {
    const int ff = f - 216, nh = ff / 36, nt = (ff % 36) / 12, ks = ff % 12;
    const int n = 96 * nh + 32 * nt + tau32(r);
    for (int j = 0; j < 8; ++j) v[j] = wo[(16 * ks + 8 * (j >> 2) + 4 * h + (j & 3)) * 192 + n];
  } else if (f < 352) {
    const int ff = f - 288, hd = ff >> 4, jt = (ff >> 3) & 1, it = (ff >> 2) & 1, q = ff & 3;
    const int i = 32 * it + tau32(r);
    for (int e = 0; e < 4; ++e) {
      const int j = 32 * jt + 16 * h + 4 * q + e;
      const unsigned b = rpb[rp_idx[i * 64 + j] * 4 + hd];
      v[2 * e] = 0; v[2 * e + 1] = (unsigned short)b;
    }
  } else return;
  u32x4 o;
  for (int j = 0; j < 4; ++j) o[j] = (unsigned)v[2 * j] | ((unsigned)v[2 * j + 1] << 16);
  *(u32x4*)(ws + (size_t)gid * 16) = o;
}

#define MFMA32(a, b, c) __builtin_amdgcn_mfma_f32_32x32x16_bf16((a), (b), (c), 0, 0, 0)
#define MFMA16(a, b, c) __builtin_amdgcn_mfma_f32_16x16x32_bf16((a), (b), (c), 0, 0, 0)

DI bf16x8 pack8(const f32x16& x, int s, float sc) {
  u32x4 p;
#pragma unroll
  for (int e = 0; e < 4; ++e) p[e] = cvtpk(x[8 * s + 2 * e] * sc, x[8 * s + 2 * e + 1] * sc);
  return __builtin_bit_cast(bf16x8, p);
}
DI bf16x8 pack8n(const f32x16& x, int s) {
  u32x4 p;
#pragma unroll
  for (int e = 0; e < 4; ++e) p[e] = cvtpk(x[8 * s + 2 * e], x[8 * s + 2 * e + 1]);
  return __builtin_bit_cast(bf16x8, p);
}

constexpr int LX = 0;
constexpr int LM = LX + 24576;
constexpr int LO = LM + 8192;
constexpr int LS = LO + 24576;
constexpr int LW = LS + 8192;
constexpr int LDS_BYTES = LW + 73728;

__global__ __launch_bounds__(256, 1)
void swin_kernel(const char* __restrict__ x, const char* __restrict__ mask, const char* __restrict__ ws, char* __restrict__ out) {
  extern __shared__ __attribute__((aligned(16))) char lds[];
  const int tid = threadIdx.x, l = tid & 63, w = __builtin_amdgcn_readfirstlane(tid >> 6), r = l & 31, h = l >> 5;

  bf16x8 wqk[3][12], wv0[12], wv1[6];
#pragma unroll
  for (int dt = 0; dt < 3; ++dt)
#pragma unroll
    for (int ks = 0; ks < 12; ++ks) wqk[dt][ks] = *(const bf16x8*)(ws + WS_WQK + ((w * 3 + dt) * 12 + ks) * 1024 + l * 16);
#pragma unroll
  for (int ks = 0; ks < 12; ++ks) wv0[ks] = *(const bf16x8*)(ws + WS_WV0 + (w * 12 + ks) * 1024 + l * 16);
#pragma unroll
  for (int ks = 0; ks < 6; ++ks) wv1[ks] = *(const bf16x8*)(ws + WS_WV1 + (w * 6 + ks) * 1024 + l * 16);
  f32x16 bias[2][2];
#pragma unroll
  for (int jt = 0; jt < 2; ++jt)
#pragma unroll
    for (int it = 0; it < 2; ++it)
#pragma unroll
      for (int q = 0; q < 4; ++q) {
        f32x4 b = *(const f32x4*)(ws + WS_BIAS + ((((w * 2 + jt) * 2 + it) * 4 + q) * 1024) + l * 16);
#pragma unroll
        for (int e = 0; e < 4; ++e) bias[jt][it][4 * q + e] = b[e];
      }
  for (int i = tid; i < 73728 / 16; i += 256) *(u32x4*)(lds + LW + i * 16) = *(const u32x4*)(ws + WS_WO + i * 16);
  bf16x8 sel[2];
  {
    const int a = r & 3, hh = (r >> 2) & 1, b = r >> 3;
#pragma unroll
    for (int s = 0; s < 2; ++s)
#pragma unroll
      for (int j = 0; j < 8; ++j) sel[s][j] = (__bf16)((hh == h && (b >> 1) == s && j == 4 * (b & 1) + a) ? 1.0f : 0.0f);
  }
  const float SCALE = 0.14433756729740643f;
  const float LOG2E = 1.4426950408889634f;

  for (int itw = 0; itw < 9; ++itw) {
    const int win = blockIdx.x + 256 * itw;
    const int wh = win / 48, ww = win % 48;
    int ybase[2], xbase[2];
    ybase[0] = wh * 8 + 4; ybase[1] = (wh * 8 + 8) % IMG;
    xbase[0] = ww * 8 + 4; xbase[1] = (ww * 8 + 8) % IMG;
    __syncthreads();
#pragma unroll
    for (int ag = 0; ag < 2; ++ag) {
      const int rho = 16 * w + 8 * ag + (l >> 3), mp = l & 7, m = mp ^ ((rho >> 1) & 7);
      const int t = w >> 1, b1 = w & 1, hh = (rho >> 2) & 1, a = rho & 3;
      const size_t src = (size_t)(ybase[t] + 2 * hh + b1) * ROWB + (size_t)(xbase[ag] + a) * 384 + 16 * m;
#pragma unroll
      for (int L = 0; L < 3; ++L)
        *(u32x4*)(lds + LX + L * 8192 + rho * 128 + mp * 16) = *(const u32x4*)(x + src + 128 * L);
      const int tok = 32 * t + tau32(rho & 31);
      *(u32x4*)(lds + LM + rho * 128 + mp * 16) = *(const u32x4*)(mask + (size_t)win * 8192 + tok * 128 + 16 * m);
    }
    __syncthreads();
    bf16x8 qf[2][3], kf[2][3], vf0[2][2], vf1[2][2];
#pragma unroll
    for (int t = 0; t < 2; ++t) {
      f32x16 acc[3], accv;
#pragma unroll
      for (int i = 0; i < 16; ++i) { acc[0][i] = 0; acc[1][i] = 0; acc[2][i] = 0; accv[i] = 0; }
      const int rho = 32 * t + r;
#pragma unroll
      for (int ks = 0; ks < 12; ++ks) {
        const int kc = 2 * ks + h;
        const bf16x8 xf = *(const bf16x8*)(lds + LX + (kc >> 3) * 8192 + rho * 128 + 16 * ((kc & 7) ^ ((rho >> 1) & 7)));
        acc[0] = MFMA32(wqk[0][ks], xf, acc[0]);
        acc[1] = MFMA32(wqk[1][ks], xf, acc[1]);
        acc[2] = MFMA32(wqk[2][ks], xf, acc[2]);
        accv = MFMA32(xf, wv0[ks], accv);
      }
      qf[t][0] = pack8(acc[0], 0, SCALE); qf[t][1] = pack8(acc[0], 1, SCALE); qf[t][2] = pack8(acc[1], 0, SCALE);
      kf[t][0] = pack8n(acc[1], 1); kf[t][1] = pack8n(acc[2], 0); kf[t][2] = pack8n(acc[2], 1);
      vf0[t][0] = pack8n(accv, 0); vf0[t][1] = pack8n(accv, 1);
#pragma unroll
      for (int s = 0; s < 2; ++s) {
        f32x4 a16 = {0, 0, 0, 0};
        const int r16 = l & 15, g = l >> 4;
        const int rr = 32 * t + 16 * s + 8 * ((r16 >> 2) & 1) + 4 * (r16 >> 3) + (r16 & 3);
#pragma unroll
        for (int ks = 0; ks < 6; ++ks) {
          const int kc = 4 * ks + g;
          const bf16x8 xf = *(const bf16x8*)(lds + LX + (kc >> 3) * 8192 + rr * 128 + 16 * ((kc & 7) ^ ((rr >> 1) & 7)));
          a16 = MFMA16(xf, wv1[ks], a16);
        }
        const unsigned a0 = cvtpk(a16[0], a16[1]), a1 = cvtpk(a16[2], a16[3]);
        const unsigned ones = 0x3f803f80u;
        auto s0 = __builtin_amdgcn_permlane16_swap(a0, ones, false, false);
        auto s1 = __builtin_amdgcn_permlane16_swap(a1, ones, false, false);
        u32x4 p = {s0[0], s1[0], s0[1], s1[1]};
        vf1[t][s] = __builtin_bit_cast(bf16x8, p);
      }
    }
#pragma unroll
    for (int it = 0; it < 2; ++it) {
      f32x16 S[2];
      const int rho = 32 * it + r;
#pragma unroll
      for (int jt = 0; jt < 2; ++jt) {
        S[jt] = bias[jt][it];
#pragma unroll
        for (int s = 0; s < 3; ++s) S[jt] = MFMA32(kf[jt][s], qf[it][s], S[jt]);
#pragma unroll
        for (int s = 0; s < 2; ++s) {
          const int ch = 4 * jt + 2 * h + s;
          const bf16x8 mf = *(const bf16x8*)(lds + LM + rho * 128 + 16 * (ch ^ ((rho >> 1) & 7)));
          S[jt] = MFMA32(sel[s], mf, S[jt]);
        }
      }
      float m = S[0][0];
#pragma unroll
      for (int i = 1; i < 16; ++i) m = fmaxf(m, S[0][i]);
#pragma unroll
      for (int i = 0; i < 16; ++i) m = fmaxf(m, S[1][i]);
      {
        auto pr = __builtin_amdgcn_permlane32_swap(__float_as_uint(m), __float_as_uint(m), false, false);
        m = fmaxf(__uint_as_float(pr[0]), __uint_as_float(pr[1]));
      }
      const float nm = -m * LOG2E;
#pragma unroll
      for (int jt = 0; jt < 2; ++jt)
#pragma unroll
        for (int i = 0; i < 16; ++i) S[jt][i] = __builtin_amdgcn_exp2f(__builtin_fmaf(S[jt][i], LOG2E, nm));
      f32x16 o0, o1;
#pragma unroll
      for (int i = 0; i < 16; ++i) { o0[i] = 0; o1[i] = 0; }
#pragma unroll
      for (int jt = 0; jt < 2; ++jt)
#pragma unroll
        for (int s = 0; s < 2; ++s) {
          const bf16x8 pf = pack8n(S[jt], s);
          o0 = MFMA32(vf0[jt][s], pf, o0);
          o1 = MFMA32(vf1[jt][s], pf, o1);
        }
      const float inv = __builtin_amdgcn_rcpf(o1[8]);
      *(bf16x8*)(lds + LO + (it * 12 + 3 * w + 0) * 1024 + l * 16) = pack8(o0, 0, inv);
      *(bf16x8*)(lds + LO + (it * 12 + 3 * w + 1) * 1024 + l * 16) = pack8(o0, 1, inv);
      *(bf16x8*)(lds + LO + (it * 12 + 3 * w + 2) * 1024 + l * 16) = pack8(o1, 0, inv);
    }
    __syncthreads();
    {
      const int it = w & 1, nh = w >> 1;
      f32x16 acc[3];
#pragma unroll
      for (int i = 0; i < 16; ++i) { acc[0][i] = 0; acc[1][i] = 0; acc[2][i] = 0; }
#pragma unroll
      for (int ks = 0; ks < 12; ++ks) {
        const bf16x8 of = *(const bf16x8*)(lds + LO + (it * 12 + ks) * 1024 + l * 16);
#pragma unroll
        for (int nt = 0; nt < 3; ++nt) {
          const bf16x8 wf = *(const bf16x8*)(lds + LW + ((nh * 3 + nt) * 12 + ks) * 1024 + l * 16);
          acc[nt] = MFMA32(wf, of, acc[nt]);
        }
      }
      char* stg = lds + LS + w * 2048;
#pragma unroll
      for (int nt = 0; nt < 3; ++nt) {
#pragma unroll
        for (int e = 0; e < 2; ++e)
          *(bf16x8*)(stg + r * 64 + 16 * ((2 * h + e) ^ ((r >> 1) & 3))) = pack8n(acc[nt], e);
        __builtin_amdgcn_s_waitcnt(0xC07F);
        __builtin_amdgcn_wave_barrier();
#pragma unroll
        for (int q = 0; q < 2; ++q) {
          const int a = (l >> 2) & 3, hh = (l >> 4) & 1, b1 = l >> 5;
          const int rp = a + 4 * hh + 8 * q + 16 * b1, c = l & 3;
          const u32x4 v = *(const u32x4*)(stg + rp * 64 + 16 * (c ^ ((rp >> 1) & 3)));
          const size_t dst = (size_t)(ybase[it] + 2 * hh + b1) * ROWB + (size_t)(xbase[q] + a) * 384 + 192 * nh + 64 * nt + 16 * c;
          *(u32x4*)(out + dst) = v;
        }
        __builtin_amdgcn_s_waitcnt(0xC07F);
        __builtin_amdgcn_wave_barrier();
      }
    }
  }
}

extern "C" void kernel_launch(void* const* d_in, const int* in_sizes, int n_in,
                              void* d_out, int out_size, void* d_ws, size_t ws_size,
                              hipStream_t stream) {
  (void)hipFuncSetAttribute((const void*)swin_kernel, hipFuncAttributeMaxDynamicSharedMemorySize, LDS_BYTES);
  prep_kernel<<<88, 256, 0, stream>>>((const unsigned short*)d_in[1], (const unsigned short*)d_in[2],
                                      (const unsigned short*)d_in[3], (const int*)d_in[4], (char*)d_ws);
  swin_kernel<<<256, 256, LDS_BYTES, stream>>>((const char*)d_in[0], (const char*)d_in[5], (const char*)d_ws, (char*)d_out);
}
```

```cpp
#include <hip/hip_runtime.h>
#include <hip/hip_bf16.h>

typedef float    f32x4  __attribute__((ext_vector_type(4)));
typedef float    f32x2  __attribute__((ext_vector_type(2)));
typedef __bf16   bf16x8 __attribute__((ext_vector_type(8)));
typedef __bf16   bf16x2 __attribute__((ext_vector_type(2)));
typedef unsigned u32x4  __attribute__((ext_vector_type(4)));
typedef unsigned u32x2  __attribute__((ext_vector_type(2)));
#define DI __device__ __forceinline__

constexpr int IMG = 384, ROWB = IMG * 192 * 2;

constexpr size_t WS_W = 0;
constexpr size_t WS_BIAS = WS_W + 4 * 72 * 1024;

__host__ __device__ inline int tok16(int r) { return 8 * ((r >> 2) & 1) + 4 * ((r >> 3) & 1) + (r & 3); }
DI unsigned cvtpk(float lo, float hi) {
  f32x2 v = {lo, hi}; bf16x2 b = __builtin_convertvector(v, bf16x2); return __builtin_bit_cast(unsigned, b);
}

__global__ void prep_kernel(const unsigned short* __restrict__ wqkv, const unsigned short* __restrict__ wo,
                            const unsigned short* __restrict__ rpb, const int* __restrict__ rp_idx, char* __restrict__ ws) {
  const int gid = blockIdx.x * blockDim.x + threadIdx.x;
  const int l = gid & 63, f = gid >> 6, r = l & 15, g = l >> 4;
  u32x4 o;
  if (f < 288) {
    const int hd = f / 72, ff = f % 72, tile = ff / 6, ks = ff % 6;
    unsigned short v[8];
    if (tile < 9) {
      const int col = 192 * (tile / 3) + 48 * hd + 16 * (tile % 3) + r;
      for (int j = 0; j < 8; ++j) v[j] = wqkv[(32 * ks + 8 * g + j) * 576 + col];
      if (tile < 3) {
        for (int j = 0; j < 8; j += 2) {
          const unsigned p = cvtpk(__uint_as_float((unsigned)v[j] << 16) * 0.14433756729740643f,
                                   __uint_as_float((unsigned)v[j + 1] << 16) * 0.14433756729740643f);
          v[j] = p & 0xffff; v[j + 1] = p >> 16;
        }
      }
    } else {
      const int nt = tile - 9;
      const int n = 48 * hd + (nt < 2 ? 8 * (r >> 2) + 4 * nt + (r & 3) : 32 + r);
      for (int j = 0; j < 8; ++j) {
        int c;
        if (ks < 4) c = 48 * ks + 16 * (j >> 2) + 4 * g + (j & 3);
        else        c = 48 * (2 * (ks - 4) + (j >> 2)) + 32 + 4 * g + (j & 3);
        v[j] = wo[c * 192 + n];
      }
    }
    for (int j = 0; j < 4; ++j) o[j] = (unsigned)v[2 * j] | ((unsigned)v[2 * j + 1] << 16);
  } else if (f < 352) {
    const int ff = f - 288, hd = ff >> 4, jt = (ff >> 2) & 3, it = ff & 3;
    const int i = 16 * it + tok16(r);
    for (int e = 0; e < 4; ++e) {
      const int j = 16 * jt + tok16(4 * g + e);
      o[e] = (unsigned)rpb[rp_idx[i * 64 + j] * 4 + hd] << 16;
    }
  } else return;
  *(u32x4*)(ws + (size_t)gid * 16) = o;
}

#define MFMA16(a, b, c) __builtin_amdgcn_mfma_f32_16x16x32_bf16(__builtin_bit_cast(bf16x8, (a)), __builtin_bit_cast(bf16x8, (b)), (c), 0, 0, 0)

constexpr int LX = 0;
constexpr int LM = LX + 24576;
constexpr int LO = LM + 8192;
constexpr int LS = LO + 24576;
constexpr int LDS_BYTES = LS + 24576;

__global__ __launch_bounds__(256, 1)
void swin_kernel(const char* __restrict__ x, const char* __restrict__ mask, const char* ws, char* out) {
  extern __shared__ __attribute__((aligned(16))) char lds[];
  const int tid = threadIdx.x, l = tid & 63, w = __builtin_amdgcn_readfirstlane(tid >> 6), r = l & 15, g = l >> 4;

  u32x4 W[9][6];
#pragma unroll
  for (int t = 0; t < 9; ++t)
#pragma unroll
    for (int ks = 0; ks < 6; ++ks) W[t][ks] = *(const u32x4*)(ws + WS_W + ((w * 12 + t) * 6 + ks) * 1024 + l * 16);
  unsigned I0, I1;
  {
    const int t = tok16(r);
    const bool on = (t >> 2) == g; const int u = t & 3;
    I0 = on ? (u == 0 ? 0x3f80u : u == 1 ? 0x3f800000u : 0u) : 0u;
    I1 = on ? (u == 2 ? 0x3f80u : u == 3 ? 0x3f800000u : 0u) : 0u;
  }
  const u32x4 ones = {0x3f803f80u, 0x3f803f80u, 0x3f803f80u, 0x3f803f80u};
  const float LOG2E = 1.4426950408889634f;
  const f32x4 zero = {0, 0, 0, 0};
  const unsigned vglob = (l >> 5) * ROWB + ((l >> 3) & 3) * 384 + 16 * ((l & 7) ^ (l >> 3));

  for (int itw = 0; itw < 9; ++itw) {
    const int win = blockIdx.x + 256 * itw;
    const int wh = win / 48, ww = win % 48;
    const int yb = (wh * 8 + 4 + 2 * w) % IMG;
    unsigned sof[2];
    sof[0] = yb * ROWB + (ww * 8 + 4) * 384; sof[1] = yb * ROWB + ((ww * 8 + 8) % IMG) * 384;
    __syncthreads();
#pragma unroll
    for (int ag = 0; ag < 2; ++ag) {
#pragma unroll
      for (int L = 0; L < 3; ++L)
        *(u32x4*)(lds + LX + L * 8192 + (16 * w + 8 * ag) * 128 + l * 16) = *(const u32x4*)(x + sof[ag] + vglob + 128 * L);
      const int R = 8 * w + 32 * ag + (l >> 3);
      *(u32x4*)(lds + LM + (8 * w + 32 * ag) * 128 + l * 16) = *(const u32x4*)(mask + (size_t)win * 8192 + R * 128 + 16 * ((l & 7) ^ ((R >> 1) & 7)));
    }
    __syncthreads();
    u32x4 Q01[4], K01[4], V[3][2]; u32x2 Q2[4], K2[4];
#pragma unroll
    for (int tt = 0; tt < 4; ++tt) {
      u32x4 xf[6];
      const int rho = 16 * tt + r;
#pragma unroll
      for (int ks = 0; ks < 6; ++ks) xf[ks] = *(const u32x4*)(lds + LX + (ks >> 1) * 8192 + rho * 128 + 16 * ((4 * (ks & 1) + g) ^ (rho & 7)));
      unsigned p[9][2];
#pragma unroll
      for (int t = 0; t < 9; ++t) {
        f32x4 acc = zero;
#pragma unroll
        for (int ks = 0; ks < 6; ++ks) acc = t < 6 ? MFMA16(W[t][ks], xf[ks], acc) : MFMA16(xf[ks], W[t][ks], acc);
        p[t][0] = cvtpk(acc[0], acc[1]); p[t][1] = cvtpk(acc[2], acc[3]);
      }
      Q01[tt] = u32x4{p[0][0], p[0][1], p[1][0], p[1][1]}; Q2[tt] = u32x2{p[2][0], p[2][1]};
      K01[tt] = u32x4{p[3][0], p[3][1], p[4][0], p[4][1]}; K2[tt] = u32x2{p[5][0], p[5][1]};
#pragma unroll
      for (int vt = 0; vt < 3; ++vt) { V[vt][tt >> 1][2 * (tt & 1)] = p[6 + vt][0]; V[vt][tt >> 1][2 * (tt & 1) + 1] = p[6 + vt][1]; }
    }
#pragma unroll
    for (int it = 0; it < 4; ++it) {
      f32x4 S[4];
      const int R = 16 * it + tok16(r);
#pragma unroll
      for (int jt = 0; jt < 4; ++jt) {
        const u32x2 m = *(const u32x2*)(lds + LM + R * 128 + 16 * ((2 * jt + (g >> 1)) ^ ((R >> 1) & 7)) + 8 * (g & 1));
        S[jt] = MFMA16(K01[jt], Q01[it], *(const f32x4*)(ws + WS_BIAS + (((w * 4 + jt) * 4 + it) * 1024) + l * 16));
        const u32x4 a = {K2[jt][0], K2[jt][1], I0, I1}, b = {Q2[it][0], Q2[it][1], m[0], m[1]};
        S[jt] = MFMA16(a, b, S[jt]);
      }
      float m = S[0][0];
#pragma unroll
      for (int i = 1; i < 16; ++i) m = fmaxf(m, S[i >> 2][i & 3]);
      {
        auto pr = __builtin_amdgcn_permlane32_swap(__float_as_uint(m), __float_as_uint(m), false, false);
        m = fmaxf(__uint_as_float(pr[0]), __uint_as_float(pr[1]));
        auto p2 = __builtin_amdgcn_permlane16_swap(__float_as_uint(m), __float_as_uint(m), false, false);
        m = fmaxf(__uint_as_float(p2[0]), __uint_as_float(p2[1]));
      }
      const float nm = -m * LOG2E;
      u32x4 P[2];
#pragma unroll
      for (int jt = 0; jt < 4; ++jt) {
        float e[4];
#pragma unroll
        for (int i = 0; i < 4; ++i) e[i] = __builtin_amdgcn_exp2f(__builtin_fmaf(S[jt][i], LOG2E, nm));
        P[jt >> 1][2 * (jt & 1)] = cvtpk(e[0], e[1]); P[jt >> 1][2 * (jt & 1) + 1] = cvtpk(e[2], e[3]);
      }
      f32x4 o[4];
#pragma unroll
      for (int vt = 0; vt < 3; ++vt) { o[vt] = MFMA16(V[vt][0], P[0], zero); o[vt] = MFMA16(V[vt][1], P[1], o[vt]); }
      o[3] = MFMA16(ones, P[0], zero); o[3] = MFMA16(ones, P[1], o[3]);
      const float inv = __builtin_amdgcn_rcpf(o[3][0]);
      u32x4 a = {cvtpk(o[0][0] * inv, o[0][1] * inv), cvtpk(o[0][2] * inv, o[0][3] * inv), cvtpk(o[1][0] * inv, o[1][1] * inv), cvtpk(o[1][2] * inv, o[1][3] * inv)};
      u32x2 b = {cvtpk(o[2][0] * inv, o[2][1] * inv), cvtpk(o[2][2] * inv, o[2][3] * inv)};
      *(u32x4*)(lds + LO + (it * 6 + w) * 1024 + l * 16) = a;
      *(u32x2*)(lds + LO + (it * 6 + 4 + (w >> 1)) * 1024 + l * 16 + 8 * (w & 1)) = b;
    }
    __syncthreads();
#pragma unroll
    for (int tt = 0; tt < 4; ++tt) {
      u32x4 of[6];
#pragma unroll
      for (int ks = 0; ks < 6; ++ks) of[ks] = *(const u32x4*)(lds + LO + (tt * 6 + ks) * 1024 + l * 16);
      unsigned p[3][2];
#pragma unroll
      for (int nt = 0; nt < 3; ++nt) {
        f32x4 acc = zero;
#pragma unroll
        for (int ks = 0; ks < 6; ++ks) acc = MFMA16(*(const u32x4*)(ws + WS_W + ((w * 12 + 9 + nt) * 6 + ks) * 1024 + l * 16), of[ks], acc);
        p[nt][0] = cvtpk(acc[0], acc[1]); p[nt][1] = cvtpk(acc[2], acc[3]);
      }
      const int rho = 16 * tt + r;
      const int c0 = 6 * w + g, c1 = 6 * w + 4 + (g >> 1);
      *(u32x4*)(lds + LS + (c0 >> 3) * 8192 + rho * 128 + 16 * ((c0 & 7) ^ (rho & 7))) = u32x4{p[0][0], p[0][1], p[1][0], p[1][1]};
      *(u32x2*)(lds + LS + (c1 >> 3) * 8192 + rho * 128 + 16 * ((c1 & 7) ^ (rho & 7)) + 8 * (g & 1)) = u32x2{p[2][0], p[2][1]};
    }
    __syncthreads();
#pragma unroll
    for (int ag = 0; ag < 2; ++ag)
#pragma unroll
      for (int L = 0; L < 3; ++L)
        *(u32x4*)(out + sof[ag] + vglob + 128 * L) = *(const u32x4*)(lds + LS + L * 8192 + (16 * w + 8 * ag) * 128 + l * 16);
  }
}

extern "C" void kernel_launch(void* const* d_in, const int* in_sizes, int n_in,
                              void* d_out, int out_size, void* d_ws, size_t ws_size,
                              hipStream_t stream) {
  (void)hipFuncSetAttribute((const void*)swin_kernel, hipFuncAttributeMaxDynamicSharedMemorySize, LDS_BYTES);
  prep_kernel<<<88, 256, 0, stream>>>((const unsigned short*)d_in[1], (const unsigned short*)d_in[2],
                                      (const unsigned short*)d_in[3], (const int*)d_in[4], (char*)d_ws);
  swin_kernel<<<256, 256, LDS_BYTES, stream>>>((const char*)d_in[0], (const char*)d_in[5], (const char*)d_ws, (char*)d_out);
}
```

```cpp
#include <hip/hip_runtime.h>
#include <hip/hip_bf16.h>

typedef unsigned u32x4 __attribute__((ext_vector_type(4)));
#define DI __device__ __forceinline__

constexpr unsigned ROWB = 384 * 192 * 2;
constexpr int LDS_BYTES = 163840;

DI unsigned tok16(unsigned r) { return 8 * ((r >> 2) & 1) + 4 * ((r >> 3) & 1) + (r & 3); }
DI u32x4 make_srd(const void* p, unsigned bytes) {
  const unsigned long long a = (unsigned long long)p;
  u32x4 d = {(unsigned)a, (unsigned)(a >> 32) & 0xffffu, bytes, 0x00020000u};
  return d;
}

__global__ __launch_bounds__(256, 1)
void swin_kernel(const char* __restrict__ x, const char* __restrict__ wqkv, const char* __restrict__ wo, const char* __restrict__ rpb,
                 const char* __restrict__ idx, const char* __restrict__ mask, char* __restrict__ out) {
  const unsigned tid = threadIdx.x, l0 = tid & 63, w = __builtin_amdgcn_readfirstlane(tid >> 6);
  const unsigned slot = ((blockIdx.x >> 3) & 31) * 4 + w, win0 = __builtin_amdgcn_readfirstlane(blockIdx.x);
  unsigned vK0, vK1 = min(2 * slot + (l0 & 1), 175u - 1) * 128;
  asm volatile("s_getpc_b64 s[24:25]\n\tglobal_load_dword v15, v15, s[24:25]" : "+{v15}"(vK1) : : "s24", "s25", "memory");
  unsigned vG = (l0 >> 5) * ROWB + ((l0 >> 3) & 3) * 384 + 16 * ((l0 & 7) ^ (l0 >> 3));
  unsigned vL16 = 16 * l0, vID = (16 * w + tok16(l0 & 15)) * 256 + 32 * ((l0 >> 4) & 1) + 16 * (l0 >> 5);
  u32x4 vJ0, vJ1, vJ2, vJ3;
  unsigned sWIN = win0, sWH = sWIN / 48, sWW = sWIN % 48, sXMW = 1024 * w, sW = w;
  unsigned sYO = 4 + 2 * (w >> 1), sXO = 4 + 4 * (w & 1);
  const unsigned y0 = 8 * sWH + sYO, x0 = 8 * sWW + sXO;
  unsigned sA0 = y0 * ROWB + (x0 == 384 ? 0 : x0) * 384, sA1 = sA0 + 4 * ROWB - (y0 >= 380 ? 384 * ROWB : 0);
  u32x4 srdX = make_srd(x, 56623104u), srdI = make_srd(idx, 16384u), srdR = make_srd(rpb, 1800u);
  unsigned long long t0, t1;
  asm volatile(
    "buffer_load_dwordx4 v[112:115], v231, s[84:87], 0 offen offset:0\n\t"
    "buffer_load_dwordx4 v[116:119], v231, s[84:87], 0 offen offset:64\n\t"
    "buffer_load_dwordx4 v[120:123], v231, s[84:87], 0 offen offset:128\n\t"
    "buffer_load_dwordx4 v[124:127], v231, s[84:87], 0 offen offset:192\n\t"
    "s_and_b32 s26, s61, 1024\n\t"
    "s_add_u32 m0, s26, 24576\n\t"
    "s_nop 0\n\t"
    "buffer_load_dwordx4 v11, s[88:91], s26 offen lds\n\t"
    "s_add_u32 m0, s61, 0\n\t"
    "s_nop 0\n\t"
    "buffer_load_dwordx4 v9, s[36:39], s48 offen nt lds\n\t"
    "s_add_u32 m0, s61, 8064\n\t"
    "s_nop 0\n\t"
    "buffer_load_dwordx4 v9, s[36:39], s48 offen offset:128 nt lds\n\t"
    "s_add_u32 m0, s61, 16128\n\t"
    "s_nop 0\n\t"
    "buffer_load_dwordx4 v9, s[36:39], s48 offen offset:256 nt lds\n\t"
    "s_add_u32 m0, s61, 4096\n\t"
    "s_nop 0\n\t"
    "buffer_load_dwordx4 v9, s[36:39], s49 offen nt lds\n\t"
    "s_add_u32 m0, s61, 12160\n\t"
    "s_nop 0\n\t"
    "buffer_load_dwordx4 v9, s[36:39], s49 offen offset:128 nt lds\n\t"
    "s_add_u32 m0, s61, 20224\n\t"
    "s_nop 0\n\t"
    "buffer_load_dwordx4 v9, s[36:39], s49 offen offset:256 nt lds\n\t"
    : "+{v9}"(vG), "+{v11}"(vL16), "+{v231}"(vID), "+{v15}"(vK1), "=&{v[112:115]}"(vJ0), "=&{v[116:119]}"(vJ1), "=&{v[120:123]}"(vJ2), "=&{v[124:127]}"(vJ3), "+{s73}"(sW), "+{s61}"(sXMW), "+{s48}"(sA0), "+{s49}"(sA1), "+{s[36:39]}"(srdX), "+{s[84:87]}"(srdI), "+{s[88:91]}"(srdR)
    :
    : "s26", "scc", "memory");
  __builtin_amdgcn_sched_barrier(0);
  {
    const unsigned lt = vL16 >> 4;
    const char* pf = (lt < 16 ? wqkv : lt < 48 ? wo : mask + (size_t)win0 * 8192) +
                     (lt < 16 ? min(slot * 14 + min(lt, 13u), 1727u) : lt < 48 ? min(slot * 5 + min(lt - 16, 4u), 575u) : 16 * w + lt - 48) * 128;
    asm volatile("global_load_dword v14, %2, off" : "=&{v14}"(vK0), "+v"(vL16) : "v"(pf) : "memory");
  }
  __builtin_amdgcn_sched_barrier(0);
  const unsigned l = vL16 >> 4, r = l & 15, g = l >> 4;
  const unsigned LO = 65536, ST0 = 98304, ST1 = 131072;
  unsigned vXE = r * 128 + 16 * (g ^ (r & 7)), vXO = vXE ^ 64;
  const unsigned Rm = tok16(r);
  unsigned vM = 24576 + Rm * 128 + 16 * ((g >> 1) ^ ((Rm >> 1) & 7)) + 8 * (g & 1);
  unsigned vOR = LO + 16 * l, vOW = LO + 16 * l + 1024 * w, vOW2 = LO + (4 + (w >> 1)) * 1024 + 16 * l + 8 * (w & 1);
  const unsigned c0 = 6 * w + g, c1 = 6 * w + 4 + (g >> 1);
  unsigned vSW = ST0 + (c0 >> 3) * 8192 + r * 128 + 16 * ((c0 & 7) ^ (r & 7));
  unsigned vSW2 = ST0 + (c1 >> 3) * 8192 + r * 128 + 16 * ((c1 & 7) ^ (r & 7)) + 8 * (g & 1);
  unsigned vSR = ST1 + 1024 * w + 16 * l;
  unsigned vGM = (8 * w + (l >> 3)) * 128 + 16 * ((l & 7) ^ (4 * (w & 1) + (l >> 4)));
  const bool on = (Rm >> 2) == g;
  const unsigned one = 0x40deu << (16 * (Rm & 1));
  unsigned vI0 = (on && !(Rm & 2)) ? one : 0u, vI1 = (on && (Rm & 2)) ? one : 0u;
  const unsigned q = r >> 2, p4 = l & 3, sz = 2 * (((q >> 1) & 1) | ((g & 1) << 1));
  const unsigned tb = 53248 + (8 * g + q) * 384 + 8 * (p4 & 1);
  unsigned vT0 = tb + 16 * ((6 * w + 0 + (p4 >> 1)) ^ sz), vT1 = tb + 16 * ((6 * w + 2 + (p4 >> 1)) ^ sz), vT2 = tb + 16 * ((6 * w + 4 + (p4 >> 1)) ^ sz);
  unsigned vU0 = vT0 + 73728, vU1 = vT1 + 73728, vU2 = vT2 + 73728;
  unsigned vTo0 = 90112 + (4 * g + q) * 384 + 16 * ((6 * w + p4) ^ sz);
  unsigned vTo1 = 90112 + (4 * g + q) * 384 + 16 * ((6 * w + 4 + (p4 >> 1)) ^ sz) + 8 * (p4 & 1);
  unsigned vTo2 = vTo0 + 36864, vTo3 = vTo1 + 36864;
  unsigned dq[3], de[3];
#pragma unroll
  for (unsigned j = 0; j < 3; ++j) {
    const unsigned i = 64 * j + l, lr = (i * 171) >> 12, pos = i - 24 * lr;
    dq[j] = lr * 1152 + 16 * (pos ^ (2 * (((lr >> 1) & 1) | ((w & 1) << 1))));
    de[j] = lr * 384 + 16 * (pos ^ (2 * ((lr >> 1) & 3)));
  }
  unsigned vD0 = dq[0], vD1 = dq[1], vD2 = dq[2], vE0 = de[0], vE1 = de[1], vE2 = de[2];
  unsigned vBW = 53248 + 2048 * w + 16 * l, vBR = 53248 + 8192 * w + 16 * l;
  u32x4 srdM = make_srd(mask, 18874368u), srdO = make_srd(out, 56623104u), srdW = make_srd(wqkv, 221184u), srdWo = make_srd(wo, 73728u);
  unsigned sN = 1, sL2E = 0x3e553b95u, sSC = 0x3e13cd3au;
  asm volatile(
    "s_getpc_b64 s[24:25]\n\t"
    "PC_%=:\n\t"
    "s_add_u32 s24, s24, TAIL_%=-PC_%=\n\t"
    "s_addc_u32 s25, s25, 0\n\t"
    "s_and_b32 s25, s25, 0xffff\n\t"
    "s_mov_b32 s18, END_%=-TAIL_%=\n\t"
    "s_mov_b32 s16, 0\n\t"
    "s_mov_b32 s17, 4096\n\t"
    "s_mul_i32 s27, s73, 3072\n\t"
    "s_waitcnt vmcnt(7)\n\t"
    "s_barrier\n\t"
    "s_mul_i32 s26, s73, 9216\n\t"
    "s_add_u32 s26, s26, 384\n\t"
    "s_add_u32 m0, s27, 90112\n\t"
    "s_nop 0\n\t"
    "buffer_load_dwordx4 v224, s[76:79], s26 offen lds\n\t"
    "s_mov_b32 s63, 24576\n\t"
    "s_mov_b32 s56, 0x05040100\n\t"
    "s_mov_b32 s57, 0x07060302\n\t"
    "v_lshl_add_u32 v112, v112, 3, s63\n\t"
    "v_lshl_add_u32 v113, v113, 3, s63\n\t"
    "v_lshl_add_u32 v114, v114, 3, s63\n\t"
    "v_lshl_add_u32 v115, v115, 3, s63\n\t"
    "ds_read_b64 v[144:145], v112 offset:0\n\t"
    "ds_read_b64 v[146:147], v113 offset:0\n\t"
    "ds_read_b64 v[148:149], v114 offset:0\n\t"
    "ds_read_b64 v[150:151], v115 offset:0\n\t"
    "v_lshl_add_u32 v116, v116, 3, s63\n\t"
    "s_add_u32 m0, s27, 91136\n\t"
    "s_nop 0\n\t"
    "buffer_load_dwordx4 v225, s[76:79], s26 offen lds\n\t"
    "v_lshl_add_u32 v117, v117, 3, s63\n\t"
    "v_lshl_add_u32 v118, v118, 3, s63\n\t"
    "v_lshl_add_u32 v119, v119, 3, s63\n\t"
    "ds_read_b64 v[152:153], v116 offset:0\n\t"
    "ds_read_b64 v[154:155], v117 offset:0\n\t"
    "ds_read_b64 v[156:157], v118 offset:0\n\t"
    "ds_read_b64 v[158:159], v119 offset:0\n\t"
    "v_lshl_add_u32 v120, v120, 3, s63\n\t"
    "v_lshl_add_u32 v121, v121, 3, s63\n\t"
    "v_lshl_add_u32 v122, v122, 3, s63\n\t"
    "v_lshl_add_u32 v123, v123, 3, s63\n\t"
    "ds_read_b64 v[160:161], v120 offset:0\n\t"
    "s_add_u32 m0, s27, 92160\n\t"
    "s_nop 0\n\t"
    "buffer_load_dwordx4 v226, s[76:79], s26 offen lds\n\t"
    "ds_read_b64 v[162:163], v121 offset:0\n\t"
    "ds_read_b64 v[164:165], v122 offset:0\n\t"
    "ds_read_b64 v[166:167], v123 offset:0\n\t"
    "v_lshl_add_u32 v124, v124, 3, s63\n\t"
    "v_lshl_add_u32 v125, v125, 3, s63\n\t"
    "v_lshl_add_u32 v126, v126, 3, s63\n\t"
    "v_lshl_add_u32 v127, v127, 3, s63\n\t"
    "ds_read_b64 v[168:169], v124 offset:0\n\t"
    "ds_read_b64 v[170:171], v125 offset:0\n\t"
    "ds_read_b64 v[172:173], v126 offset:0\n\t"
    "ds_read_b64 v[174:175], v127 offset:0\n\t"
    "s_waitcnt lgkmcnt(14)\n\t"
    "v_perm_b32 v48, v146, v144, s56\n\t"
    "s_mul_i32 s26, s73, 9216\n\t"
    "s_add_u32 s26, s26, 37248\n\t"
    "s_add_u32 m0, s27, 102400\n\t"
    "s_nop 0\n\t"
    "buffer_load_dwordx4 v224, s[76:79], s26 offen lds\n\t"
    "s_waitcnt lgkmcnt(12)\n\t"
    "v_perm_b32 v49, v150, v148, s56\n\t"
    "s_waitcnt lgkmcnt(10)\n\t"
    "v_perm_b32 v50, v154, v152, s56\n\t"
    "s_waitcnt lgkmcnt(8)\n\t"
    "v_perm_b32 v51, v158, v156, s56\n\t"
    "ds_write_b128 v251, v[48:51] offset:0\n\t"
    "v_perm_b32 v56, v146, v144, s57\n\t"
    "v_perm_b32 v57, v150, v148, s57\n\t"
    "v_perm_b32 v58, v154, v152, s57\n\t"
    "v_perm_b32 v59, v158, v156, s57\n\t"
    "ds_write_b128 v251, v[56:59] offset:8192\n\t"
    "v_perm_b32 v64, v147, v145, s56\n\t"
    "v_perm_b32 v65, v151, v149, s56\n\t"
    "v_perm_b32 v66, v155, v153, s56\n\t"
    "s_add_u32 m0, s27, 103424\n\t"
    "s_nop 0\n\t"
    "buffer_load_dwordx4 v225, s[76:79], s26 offen lds\n\t"
    "v_perm_b32 v67, v159, v157, s56\n\t"
    "ds_write_b128 v251, v[64:67] offset:16384\n\t"
    "v_perm_b32 v72, v147, v145, s57\n\t"
    "v_perm_b32 v73, v151, v149, s57\n\t"
    "v_perm_b32 v74, v155, v153, s57\n\t"
    "v_perm_b32 v75, v159, v157, s57\n\t"
    "ds_write_b128 v251, v[72:75] offset:24576\n\t"
    "s_waitcnt lgkmcnt(10)\n\t"
    "v_perm_b32 v52, v162, v160, s56\n\t"
    "s_waitcnt lgkmcnt(8)\n\t"
    "v_perm_b32 v53, v166, v164, s56\n\t"
    "s_waitcnt lgkmcnt(6)\n\t"
    "v_perm_b32 v54, v170, v168, s56\n\t"
    "s_waitcnt lgkmcnt(4)\n\t"
    "v_perm_b32 v55, v174, v172, s56\n\t"
    "ds_write_b128 v251, v[52:55] offset:1024\n\t"
    "s_add_u32 m0, s27, 104448\n\t"
    "s_nop 0\n\t"
    "buffer_load_dwordx4 v226, s[76:79], s26 offen lds\n\t"
    "v_perm_b32 v60, v162, v160, s57\n\t"
    "v_perm_b32 v61, v166, v164, s57\n\t"
    "v_perm_b32 v62, v170, v168, s57\n\t"
    "v_perm_b32 v63, v174, v172, s57\n\t"
    "ds_write_b128 v251, v[60:63] offset:9216\n\t"
    "v_perm_b32 v68, v163, v161, s56\n\t"
    "v_perm_b32 v69, v167, v165, s56\n\t"
    "v_perm_b32 v70, v171, v169, s56\n\t"
    "v_perm_b32 v71, v175, v173, s56\n\t"
    "ds_write_b128 v251, v[68:71] offset:17408\n\t"
    "v_perm_b32 v76, v163, v161, s57\n\t"
    "v_perm_b32 v77, v167, v165, s57\n\t"
    "v_perm_b32 v78, v171, v169, s57\n\t"
    "v_perm_b32 v79, v175, v173, s57\n\t"
    "ds_write_b128 v251, v[76:79] offset:25600\n\t"
    "s_waitcnt lgkmcnt(0)\n\t"
    "s_barrier\n\t"
    "ds_read_b128 v[144:147], v255 offset:0\n\t"
    "ds_read_b128 v[148:151], v255 offset:1024\n\t"
    "ds_read_b128 v[152:155], v255 offset:2048\n\t"
    "ds_read_b128 v[156:159], v255 offset:3072\n\t"
    "ds_read_b128 v[160:163], v255 offset:4096\n\t"
    "ds_read_b128 v[164:167], v255 offset:5120\n\t"
    "ds_read_b128 v[168:171], v255 offset:6144\n\t"
    "ds_read_b128 v[172:175], v255 offset:7168\n\t"
    "v_mov_b32 v134, v130\n\t"
    "v_mov_b32 v135, v131\n\t"
    "v_mov_b32 v138, v130\n\t"
    "v_mov_b32 v139, v131\n\t"
    "v_mov_b32 v142, v130\n\t"
    "v_mov_b32 v143, v131\n\t"
    "s_mov_b32 s54, 0x40ddb3d7\n\t"
    "s_mov_b32 s55, 0x40ddb3d7\n\t"
    "s_waitcnt lgkmcnt(7)\n\t"
    "v_lshlrev_b32 v48, 16, v144\n\t"
    "v_and_b32 v49, 0xffff0000, v144\n\t"
    "v_lshlrev_b32 v50, 16, v145\n\t"
    "v_and_b32 v51, 0xffff0000, v145\n\t"
    "v_lshlrev_b32 v64, 16, v146\n\t"
    "v_and_b32 v65, 0xffff0000, v146\n\t"
    "v_lshlrev_b32 v66, 16, v147\n\t"
    "v_and_b32 v67, 0xffff0000, v147\n\t"
    "v_pk_mul_f32 v[48:49], v[48:49], s[54:55]\n\t"
    "v_pk_mul_f32 v[50:51], v[50:51], s[54:55]\n\t"
    "v_pk_mul_f32 v[64:65], v[64:65], s[54:55]\n\t"
    "v_pk_mul_f32 v[66:67], v[66:67], s[54:55]\n\t"
    "s_waitcnt lgkmcnt(6)\n\t"
    "v_lshlrev_b32 v80, 16, v148\n\t"
    "v_and_b32 v81, 0xffff0000, v148\n\t"
    "v_lshlrev_b32 v82, 16, v149\n\t"
    "v_and_b32 v83, 0xffff0000, v149\n\t"
    "v_lshlrev_b32 v96, 16, v150\n\t"
    "v_and_b32 v97, 0xffff0000, v150\n\t"
    "v_lshlrev_b32 v98, 16, v151\n\t"
    "v_and_b32 v99, 0xffff0000, v151\n\t"
    "v_pk_mul_f32 v[80:81], v[80:81], s[54:55]\n\t"
    "v_pk_mul_f32 v[82:83], v[82:83], s[54:55]\n\t"
    "v_pk_mul_f32 v[96:97], v[96:97], s[54:55]\n\t"
    "v_pk_mul_f32 v[98:99], v[98:99], s[54:55]\n\t"
    "s_waitcnt lgkmcnt(5)\n\t"
    "v_lshlrev_b32 v52, 16, v152\n\t"
    "v_and_b32 v53, 0xffff0000, v152\n\t"
    "v_lshlrev_b32 v54, 16, v153\n\t"
    "v_and_b32 v55, 0xffff0000, v153\n\t"
    "v_lshlrev_b32 v68, 16, v154\n\t"
    "v_and_b32 v69, 0xffff0000, v154\n\t"
    "v_lshlrev_b32 v70, 16, v155\n\t"
    "v_and_b32 v71, 0xffff0000, v155\n\t"
    "v_pk_mul_f32 v[52:53], v[52:53], s[54:55]\n\t"
    "v_pk_mul_f32 v[54:55], v[54:55], s[54:55]\n\t"
    "v_pk_mul_f32 v[68:69], v[68:69], s[54:55]\n\t"
    "v_pk_mul_f32 v[70:71], v[70:71], s[54:55]\n\t"
    "s_waitcnt lgkmcnt(4)\n\t"
    "v_lshlrev_b32 v84, 16, v156\n\t"
    "v_and_b32 v85, 0xffff0000, v156\n\t"
    "v_lshlrev_b32 v86, 16, v157\n\t"
    "v_and_b32 v87, 0xffff0000, v157\n\t"
    "v_lshlrev_b32 v100, 16, v158\n\t"
    "v_and_b32 v101, 0xffff0000, v158\n\t"
    "v_lshlrev_b32 v102, 16, v159\n\t"
    "v_and_b32 v103, 0xffff0000, v159\n\t"
    "v_pk_mul_f32 v[84:85], v[84:85], s[54:55]\n\t"
    "v_pk_mul_f32 v[86:87], v[86:87], s[54:55]\n\t"
    "v_pk_mul_f32 v[100:101], v[100:101], s[54:55]\n\t"
    "v_pk_mul_f32 v[102:103], v[102:103], s[54:55]\n\t"
    "s_waitcnt lgkmcnt(3)\n\t"
    "v_lshlrev_b32 v56, 16, v160\n\t"
    "v_and_b32 v57, 0xffff0000, v160\n\t"
    "v_lshlrev_b32 v58, 16, v161\n\t"
    "v_and_b32 v59, 0xffff0000, v161\n\t"
    "v_lshlrev_b32 v72, 16, v162\n\t"
    "v_and_b32 v73, 0xffff0000, v162\n\t"
    "v_lshlrev_b32 v74, 16, v163\n\t"
    "v_and_b32 v75, 0xffff0000, v163\n\t"
    "v_pk_mul_f32 v[56:57], v[56:57], s[54:55]\n\t"
    "v_pk_mul_f32 v[58:59], v[58:59], s[54:55]\n\t"
    "v_pk_mul_f32 v[72:73], v[72:73], s[54:55]\n\t"
    "v_pk_mul_f32 v[74:75], v[74:75], s[54:55]\n\t"
    "s_waitcnt lgkmcnt(2)\n\t"
    "v_lshlrev_b32 v88, 16, v164\n\t"
    "v_and_b32 v89, 0xffff0000, v164\n\t"
    "v_lshlrev_b32 v90, 16, v165\n\t"
    "v_and_b32 v91, 0xffff0000, v165\n\t"
    "v_lshlrev_b32 v104, 16, v166\n\t"
    "v_and_b32 v105, 0xffff0000, v166\n\t"
    "v_lshlrev_b32 v106, 16, v167\n\t"
    "v_and_b32 v107, 0xffff0000, v167\n\t"
    "v_pk_mul_f32 v[88:89], v[88:89], s[54:55]\n\t"
    "v_pk_mul_f32 v[90:91], v[90:91], s[54:55]\n\t"
    "v_pk_mul_f32 v[104:105], v[104:105], s[54:55]\n\t"
    "v_pk_mul_f32 v[106:107], v[106:107], s[54:55]\n\t"
    "s_waitcnt lgkmcnt(1)\n\t"
    "v_lshlrev_b32 v60, 16, v168\n\t"
    "v_and_b32 v61, 0xffff0000, v168\n\t"
    "v_lshlrev_b32 v62, 16, v169\n\t"
    "v_and_b32 v63, 0xffff0000, v169\n\t"
    "v_lshlrev_b32 v76, 16, v170\n\t"
    "v_and_b32 v77, 0xffff0000, v170\n\t"
    "v_lshlrev_b32 v78, 16, v171\n\t"
    "v_and_b32 v79, 0xffff0000, v171\n\t"
    "v_pk_mul_f32 v[60:61], v[60:61], s[54:55]\n\t"
    "v_pk_mul_f32 v[62:63], v[62:63], s[54:55]\n\t"
    "v_pk_mul_f32 v[76:77], v[76:77], s[54:55]\n\t"
    "v_pk_mul_f32 v[78:79], v[78:79], s[54:55]\n\t"
    "s_waitcnt lgkmcnt(0)\n\t"
    "v_lshlrev_b32 v92, 16, v172\n\t"
    "v_and_b32 v93, 0xffff0000, v172\n\t"
    "v_lshlrev_b32 v94, 16, v173\n\t"
    "v_and_b32 v95, 0xffff0000, v173\n\t"
    "v_lshlrev_b32 v108, 16, v174\n\t"
    "v_and_b32 v109, 0xffff0000, v174\n\t"
    "v_lshlrev_b32 v110, 16, v175\n\t"
    "v_and_b32 v111, 0xffff0000, v175\n\t"
    "v_pk_mul_f32 v[92:93], v[92:93], s[54:55]\n\t"
    "v_pk_mul_f32 v[94:95], v[94:95], s[54:55]\n\t"
    "v_pk_mul_f32 v[108:109], v[108:109], s[54:55]\n\t"
    "v_pk_mul_f32 v[110:111], v[110:111], s[54:55]\n\t"
    "s_mul_i32 s26, s73, 9216\n\t"
    "s_add_u32 s26, s26, 74112\n\t"
    "s_add_u32 m0, s27, 114688\n\t"
    "s_nop 0\n\t"
    "buffer_load_dwordx4 v224, s[76:79], s26 offen lds\n\t"
    "s_add_u32 m0, s27, 115712\n\t"
    "s_nop 0\n\t"
    "buffer_load_dwordx4 v225, s[76:79], s26 offen lds\n\t"
    "s_add_u32 m0, s27, 116736\n\t"
    "s_nop 0\n\t"
    "buffer_load_dwordx4 v226, s[76:79], s26 offen lds\n\t"
    "s_lshl_b32 s56, s72, 13\n\t"
    "s_add_u32 s57, s56, 4096\n\t"
    "s_waitcnt vmcnt(10)\n\t"
    "s_barrier\n\t"
    "s_mul_i32 s26, s73, 9216\n\t"
    "s_add_u32 s26, s26, 110976\n\t"
    "s_add_u32 m0, s27, 126976\n\t"
    "s_nop 0\n\t"
    "buffer_load_dwordx4 v224, s[76:79], s26 offen lds\n\t"
    "ds_read_b128 a[216:219], v0 offset:0\n\t"
    "ds_read_b128 a[220:223], v1 offset:0\n\t"
    "s_add_u32 m0, s27, 128000\n\t"
    "s_nop 0\n\t"
    "buffer_load_dwordx4 v225, s[76:79], s26 offen lds\n\t"
    "ds_read_b128 a[224:227], v0 offset:8192\n\t"
    "ds_read_b128 a[240:243], v0 offset:2048\n\t"
    "s_add_u32 m0, s27, 129024\n\t"
    "s_nop 0\n\t"
    "buffer_load_dwordx4 v226, s[76:79], s26 offen lds\n\t"
    "ds_read_b128 a[244:247], v1 offset:2048\n\t"
    "ds_read_b128 a[248:251], v0 offset:10240\n\t"
    "s_mul_i32 s26, s73, 9216\n\t"
    "s_add_u32 s26, s26, 147840\n\t"
    "s_add_u32 m0, s27, 139264\n\t"
    "s_nop 0\n\t"
    "buffer_load_dwordx4 v224, s[76:79], s26 offen lds\n\t"
    "ds_read_b128 v[176:179], v0 offset:4096\n\t"
    "ds_read_b128 v[180:183], v1 offset:4096\n\t"
    "s_add_u32 m0, s27, 140288\n\t"
    "s_nop 0\n\t"
    "buffer_load_dwordx4 v225, s[76:79], s26 offen lds\n\t"
    "ds_read_b128 v[184:187], v0 offset:12288\n\t"
    "ds_read_b128 v[200:203], v0 offset:6144\n\t"
    "ds_read_b128 v[204:207], v1 offset:6144\n\t"
    "ds_read_b128 v[208:211], v0 offset:14336\n\t"
    "s_waitcnt vmcnt(5)\n\t"
    "s_barrier\n\t"
    "ds_read_b64_tr_b16 a[72:73], v24 offset:36864\n\t"
    "ds_read_b64_tr_b16 a[74:75], v24 offset:38400\n\t"
    "ds_read_b64_tr_b16 a[76:77], v24 offset:49152\n\t"
    "ds_read_b64_tr_b16 a[78:79], v24 offset:50688\n\t"
    "ds_read_b64_tr_b16 a[80:81], v24 offset:61440\n\t"
    "ds_read_b64_tr_b16 a[82:83], v24 offset:62976\n\t"
    "ds_read_b64_tr_b16 a[96:97], v25 offset:36864\n\t"
    "ds_read_b64_tr_b16 a[98:99], v25 offset:38400\n\t"
    "ds_read_b64_tr_b16 a[100:101], v25 offset:49152\n\t"
    "ds_read_b64_tr_b16 a[102:103], v25 offset:50688\n\t"
    "ds_read_b64_tr_b16 a[104:105], v25 offset:61440\n\t"
    "ds_read_b64_tr_b16 a[106:107], v25 offset:62976\n\t"
    "ds_read_b64_tr_b16 a[120:121], v26 offset:36864\n\t"
    "ds_read_b64_tr_b16 a[122:123], v26 offset:38400\n\t"
    "ds_read_b64_tr_b16 a[124:125], v26 offset:49152\n\t"
    "ds_read_b64_tr_b16 a[126:127], v26 offset:50688\n\t"
    "ds_read_b64_tr_b16 a[128:129], v26 offset:61440\n\t"
    "ds_read_b64_tr_b16 a[130:131], v26 offset:62976\n\t"
    "s_add_u32 m0, s27, 141312\n\t"
    "s_waitcnt lgkmcnt(15)\n\t"
    "v_mfma_f32_16x16x32_bf16 v[144:147], a[72:75], a[216:219], 0\n\t"
    "s_add_u32 s59, s59, 16\n\t"
    "buffer_load_dwordx4 v226, s[76:79], s26 offen lds\n\t"
    "s_waitcnt lgkmcnt(14)\n\t"
    "v_mfma_f32_16x16x32_bf16 v[144:147], a[76:79], a[220:223], v[144:147]\n\t"
    "s_waitcnt lgkmcnt(12)\n\t"
    "s_mul_i32 s26, s73, 9216\n\t"
    "ds_read_b128 a[228:231], v1 offset:8192\n\t"
    "ds_read_b128 a[232:235], v0 offset:16384\n\t"
    "v_mfma_f32_16x16x32_bf16 v[144:147], a[80:83], a[224:227], v[144:147]\n\t"
    "s_waitcnt lgkmcnt(12)\n\t"
    "s_add_u32 s26, s26, 184704\n\t"
    "ds_read_b128 a[236:239], v1 offset:16384\n\t"
    "ds_read_b128 a[252:255], v1 offset:10240\n\t"
    "v_mfma_f32_16x16x32_bf16 v[148:151], a[96:99], a[216:219], 0\n\t"
    "s_waitcnt lgkmcnt(12)\n\t"
    "s_add_u32 m0, s27, 151552\n\t"
    "ds_read_b128 v[16:19], v0 offset:18432\n\t"
    "ds_read_b128 v[20:23], v1 offset:18432\n\t"
    "v_mfma_f32_16x16x32_bf16 v[148:151], a[100:103], a[220:223], v[148:151]\n\t"
    "s_cmp_ge_u32 s59, 48\n\t"
    "s_cselect_b32 s63, 48, 0\n\t"
    "s_addc_u32 s58, s58, 5\n\t"
    "buffer_load_dwordx4 v224, s[76:79], s26 offen lds\n\t"
    "s_waitcnt lgkmcnt(12)\n\t"
    "v_mfma_f32_16x16x32_bf16 v[148:151], a[104:107], a[224:227], v[148:151]\n\t"
    "s_waitcnt lgkmcnt(10)\n\t"
    "s_add_u32 m0, s27, 152576\n\t"
    "ds_read_b128 v[188:191], v1 offset:12288\n\t"
    "ds_read_b128 v[192:195], v0 offset:20480\n\t"
    "v_mfma_f32_16x16x32_bf16 v[152:155], a[120:123], a[216:219], 0\n\t"
    "s_waitcnt lgkmcnt(10)\n\t"
    "s_sub_u32 s59, s59, s63\n\t"
    "ds_read_b128 v[196:199], v1 offset:20480\n\t"
    "ds_read_b128 v[212:215], v1 offset:14336\n\t"
    "v_mfma_f32_16x16x32_bf16 v[152:155], a[124:127], a[220:223], v[152:155]\n\t"
    "s_waitcnt lgkmcnt(10)\n\t"
    "s_lshl_b32 s64, s59, 3\n\t"
    "ds_read_b128 v[216:219], v0 offset:22528\n\t"
    "ds_read_b128 v[220:223], v1 offset:22528\n\t"
    "v_mfma_f32_16x16x32_bf16 v[152:155], a[128:131], a[224:227], v[152:155]\n\t"
    "s_lshl_b32 s63, s58, 3\n\t"
    "buffer_load_dwordx4 v225, s[76:79], s26 offen lds\n\t"
    "v_mfma_f32_16x16x32_bf16 v[156:159], a[72:75], a[240:243], 0\n\t"
    "s_add_u32 m0, s27, 153600\n\t"
    "v_mfma_f32_16x16x32_bf16 v[156:159], a[76:79], a[244:247], v[156:159]\n\t"
    "s_add_u32 s64, s64, s66\n\t"
    "v_mfma_f32_16x16x32_bf16 v[156:159], a[80:83], a[248:251], v[156:159]\n\t"
    "s_add_u32 s63, s63, s65\n\t"
    "v_mfma_f32_16x16x32_bf16 v[160:163], a[96:99], a[240:243], 0\n\t"
    "s_cmp_eq_u32 s64, 384\n\t"
    "s_cselect_b32 s64, 0, s64\n\t"
    "buffer_load_dwordx4 v226, s[76:79], s26 offen lds\n\t"
    "v_mfma_f32_16x16x32_bf16 v[160:163], a[100:103], a[244:247], v[160:163]\n\t"
    "s_mul_i32 s26, s73, 9216\n\t"
    "v_mfma_f32_16x16x32_bf16 v[160:163], a[104:107], a[248:251], v[160:163]\n\t"
    "s_add_u32 s26, s26, 0\n\t"
    "v_mfma_f32_16x16x32_bf16 v[164:167], a[120:123], a[240:243], 0\n\t"
    "s_add_u32 m0, s27, 53248\n\t"
    "v_mfma_f32_16x16x32_bf16 v[164:167], a[124:127], a[244:247], v[164:167]\n\t"
    "s_mul_i32 s64, s64, 384\n\t"
    "buffer_load_dwordx4 v224, s[76:79], s26 offen lds\n\t"
    "v_mfma_f32_16x16x32_bf16 v[164:167], a[128:131], a[248:251], v[164:167]\n\t"
    "s_add_u32 m0, s27, 54272\n\t"
    "v_mfma_f32_16x16x32_bf16 v[168:171], a[72:75], v[176:179], 0\n\t"
    "s_mul_i32 s68, s63, 147456\n\t"
    "v_mfma_f32_16x16x32_bf16 v[168:171], a[76:79], v[180:183], v[168:171]\n\t"
    "s_mov_b64 s[54:55], s[52:53]\n\t"
    "v_mfma_f32_16x16x32_bf16 v[168:171], a[80:83], v[184:187], v[168:171]\n\t"
    "s_add_u32 s68, s68, s64\n\t"
    "buffer_load_dwordx4 v225, s[76:79], s26 offen lds\n\t"
    "v_mfma_f32_16x16x32_bf16 v[172:175], a[96:99], v[176:179], 0\n\t"
    "s_add_u32 m0, s27, 55296\n\t"
    "v_mfma_f32_16x16x32_bf16 v[172:175], a[100:103], v[180:183], v[172:175]\n\t"
    "s_mov_b64 s[52:53], s[50:51]\n\t"
    "v_mfma_f32_16x16x32_bf16 v[172:175], a[104:107], v[184:187], v[172:175]\n\t"
    "s_cmp_ge_u32 s63, 380\n\t"
    "s_cselect_b32 s63, 56623104, 0\n\t"
    "v_mfma_f32_16x16x32_bf16 v[232:235], a[120:123], v[176:179], 0\n\t"
    "s_add_u32 s69, s68, 589824\n\t"
    "buffer_load_dwordx4 v226, s[76:79], s26 offen lds\n\t"
    "v_mfma_f32_16x16x32_bf16 v[232:235], a[124:127], v[180:183], v[232:235]\n\t"
    "s_mul_i32 s26, s73, 9216\n\t"
    "v_mfma_f32_16x16x32_bf16 v[232:235], a[128:131], v[184:187], v[232:235]\n\t"
    "s_add_u32 s26, s26, 36864\n\t"
    "v_mfma_f32_16x16x32_bf16 v[236:239], a[72:75], v[200:203], 0\n\t"
    "s_add_u32 m0, s27, 65536\n\t"
    "v_mfma_f32_16x16x32_bf16 v[236:239], a[76:79], v[204:207], v[236:239]\n\t"
    "s_mov_b64 s[50:51], s[48:49]\n\t"
    "buffer_load_dwordx4 v224, s[76:79], s26 offen lds\n\t"
    "v_mfma_f32_16x16x32_bf16 v[236:239], a[80:83], v[208:211], v[236:239]\n\t"
    "s_add_u32 m0, s27, 66560\n\t"
    "v_mfma_f32_16x16x32_bf16 v[240:243], a[96:99], v[200:203], 0\n\t"
    "s_waitcnt vmcnt(4)\n\t"
    "s_barrier\n\t"
    "ds_read_b64_tr_b16 a[84:85], v252 offset:0\n\t"
    "ds_read_b64_tr_b16 a[86:87], v252 offset:1536\n\t"
    "v_mfma_f32_16x16x32_bf16 v[240:243], a[100:103], v[204:207], v[240:243]\n\t"
    "s_sub_u32 s69, s69, s63\n\t"
    "ds_read_b64_tr_b16 a[88:89], v252 offset:12288\n\t"
    "ds_read_b64_tr_b16 a[90:91], v252 offset:13824\n\t"
    "v_mfma_f32_16x16x32_bf16 v[240:243], a[104:107], v[208:211], v[240:243]\n\t"
    "s_mov_b64 s[48:49], s[68:69]\n\t"
    "buffer_load_dwordx4 v225, s[76:79], s26 offen lds\n\t"
    "v_mfma_f32_16x16x32_bf16 v[244:247], a[120:123], v[200:203], 0\n\t"
    "s_add_u32 m0, s27, 67584\n\t"
    "ds_read_b64_tr_b16 a[92:93], v252 offset:24576\n\t"
    "ds_read_b64_tr_b16 a[94:95], v252 offset:26112\n\t"
    "v_mfma_f32_16x16x32_bf16 v[244:247], a[124:127], v[204:207], v[244:247]\n\t"
    "s_add_u32 s72, s72, 256\n\t"
    "ds_read_b64_tr_b16 a[108:109], v253 offset:0\n\t"
    "ds_read_b64_tr_b16 a[110:111], v253 offset:1536\n\t"
    "v_mfma_f32_16x16x32_bf16 v[244:247], a[128:131], v[208:211], v[244:247]\n\t"
    "s_lshl_b32 s70, s72, 13\n\t"
    "ds_read_b64_tr_b16 a[112:113], v253 offset:12288\n\t"
    "ds_read_b64_tr_b16 a[114:115], v253 offset:13824\n\t"
    "ds_read_b64_tr_b16 a[116:117], v253 offset:24576\n\t"
    "buffer_load_dwordx4 v226, s[76:79], s26 offen lds\n\t"
    "s_mul_i32 s26, s73, 9216\n\t"
    "s_waitcnt lgkmcnt(9)\n\t"
    "v_mfma_f32_16x16x32_bf16 v[144:147], a[84:87], a[228:231], v[144:147]\n\t"
    "s_waitcnt lgkmcnt(7)\n\t"
    "s_add_u32 s26, s26, 73728\n\t"
    "ds_read_b64_tr_b16 a[118:119], v253 offset:26112\n\t"
    "ds_read_b64_tr_b16 a[132:133], v254 offset:0\n\t"
    "v_mfma_f32_16x16x32_bf16 v[144:147], a[88:91], a[232:235], v[144:147]\n\t"
    "s_add_u32 m0, s27, 77824\n\t"
    "ds_read_b64_tr_b16 a[134:135], v254 offset:1536\n\t"
    "ds_read_b64_tr_b16 a[136:137], v254 offset:12288\n\t"
    "ds_read_b64_tr_b16 a[138:139], v254 offset:13824\n\t"
    "ds_read_b64_tr_b16 a[140:141], v254 offset:24576\n\t"
    "ds_read_b64_tr_b16 a[142:143], v254 offset:26112\n\t"
    "s_add_u32 s71, s70, 4096\n\t"
    "s_add_u32 s59, s59, 16\n\t"
    "s_waitcnt lgkmcnt(12)\n\t"
    "v_mfma_f32_16x16x32_bf16 v[144:147], a[92:95], a[236:239], v[144:147]\n\t"
    "s_cmp_ge_u32 s59, 48\n\t"
    "s_cselect_b32 s63, 48, 0\n\t"
    "s_addc_u32 s58, s58, 5\n\t"
    "buffer_load_dwordx4 v224, s[76:79], s26 offen lds\n\t"
    "s_waitcnt lgkmcnt(10)\n\t"
    "v_mfma_f32_16x16x32_bf16 v[148:151], a[108:111], a[228:231], v[148:151]\n\t"
    "s_add_u32 m0, s27, 78848\n\t"
    "s_waitcnt lgkmcnt(8)\n\t"
    "v_mfma_f32_16x16x32_bf16 v[148:151], a[112:115], a[232:235], v[148:151]\n\t"
    "s_sub_u32 s59, s59, s63\n\t"
    "s_lshl_b32 s64, s59, 3\n\t"
    "s_lshl_b32 s63, s58, 3\n\t"
    "v_cvt_pk_bf16_f32 v112, v144, v145\n\t"
    "v_cvt_pk_bf16_f32 v113, v146, v147\n\t"
    "s_add_u32 s64, s64, s66\n\t"
    "s_waitcnt lgkmcnt(6)\n\t"
    "v_mfma_f32_16x16x32_bf16 v[148:151], a[116:119], a[236:239], v[148:151]\n\t"
    "s_add_u32 s63, s63, s65\n\t"
    "buffer_load_dwordx4 v225, s[76:79], s26 offen lds\n\t"
    "s_waitcnt lgkmcnt(4)\n\t"
    "v_mfma_f32_16x16x32_bf16 v[152:155], a[132:135], a[228:231], v[152:155]\n\t"
    "s_add_u32 m0, s27, 79872\n\t"
    "s_waitcnt lgkmcnt(2)\n\t"
    "v_mfma_f32_16x16x32_bf16 v[152:155], a[136:139], a[232:235], v[152:155]\n\t"
    "s_cmp_eq_u32 s64, 384\n\t"
    "s_cselect_b32 s64, 0, s64\n\t"
    "s_waitcnt lgkmcnt(0)\n\t"
    "v_mfma_f32_16x16x32_bf16 v[152:155], a[140:143], a[236:239], v[152:155]\n\t"
    "s_mul_i32 s64, s64, 384\n\t"
    "v_cvt_pk_bf16_f32 v114, v148, v149\n\t"
    "v_cvt_pk_bf16_f32 v115, v150, v151\n\t"
    "v_mfma_f32_16x16x32_bf16 v[156:159], a[84:87], a[252:255], v[156:159]\n\t"
    "s_mul_i32 s68, s63, 147456\n\t"
    "buffer_load_dwordx4 v226, s[76:79], s26 offen lds\n\t"
    "v_mfma_f32_16x16x32_bf16 v[156:159], a[88:91], v[16:19], v[156:159]\n\t"
    "s_mul_i32 s26, s73, 9216\n\t"
    "v_cvt_pk_bf16_f32 v128, v152, v153\n\t"
    "v_cvt_pk_bf16_f32 v129, v154, v155\n\t"
    "v_mfma_f32_16x16x32_bf16 v[156:159], a[92:95], v[20:23], v[156:159]\n\t"
    "s_add_u32 s26, s26, 110592\n\t"
    "v_mfma_f32_16x16x32_bf16 v[160:163], a[108:111], a[252:255], v[160:163]\n\t"
    "s_add_u32 m0, s27, 90112\n\t"
    "v_mfma_f32_16x16x32_bf16 v[160:163], a[112:115], v[16:19], v[160:163]\n\t"
    "s_add_u32 s72, s72, 256\n\t"
    "buffer_load_dwordx4 v224, s[76:79], s26 offen lds\n\t"
    "v_mfma_f32_16x16x32_bf16 v[160:163], a[116:119], v[20:23], v[160:163]\n\t"
    "s_add_u32 m0, s27, 91136\n\t"
    "v_cvt_pk_bf16_f32 v116, v156, v157\n\t"
    "v_cvt_pk_bf16_f32 v117, v158, v159\n\t"
    "v_mfma_f32_16x16x32_bf16 v[164:167], a[132:135], a[252:255], v[164:167]\n\t"
    "s_add_u32 s68, s68, s64\n\t"
    "v_mfma_f32_16x16x32_bf16 v[164:167], a[136:139], v[16:19], v[164:167]\n\t"
    "s_cmp_ge_u32 s63, 380\n\t"
    "s_cselect_b32 s63, 56623104, 0\n\t"
    "v_mfma_f32_16x16x32_bf16 v[164:167], a[140:143], v[20:23], v[164:167]\n\t"
    "s_add_u32 s69, s68, 589824\n\t"
    "buffer_load_dwordx4 v225, s[76:79], s26 offen lds\n\t"
    "v_mfma_f32_16x16x32_bf16 v[168:171], a[84:87], v[188:191], v[168:171]\n\t"
    "s_add_u32 m0, s27, 92160\n\t"
    "v_cvt_pk_bf16_f32 v118, v160, v161\n\t"
    "v_cvt_pk_bf16_f32 v119, v162, v163\n\t"
    "v_mfma_f32_16x16x32_bf16 v[168:171], a[88:91], v[192:195], v[168:171]\n\t"
    "s_mov_b32 s46, 0\n\t"
    "v_cvt_pk_bf16_f32 v132, v164, v165\n\t"
    "v_cvt_pk_bf16_f32 v133, v166, v167\n\t"
    "v_mfma_f32_16x16x32_bf16 v[168:171], a[92:95], v[196:199], v[168:171]\n\t"
    "s_sub_u32 s69, s69, s63\n\t"
    "v_mfma_f32_16x16x32_bf16 v[172:175], a[108:111], v[188:191], v[172:175]\n\t"
    "buffer_load_dwordx4 v226, s[76:79], s26 offen lds\n\t"
    "v_mfma_f32_16x16x32_bf16 v[172:175], a[112:115], v[192:195], v[172:175]\n\t"
    "s_mul_i32 s26, s73, 9216\n\t"
    "v_mfma_f32_16x16x32_bf16 v[172:175], a[116:119], v[196:199], v[172:175]\n\t"
    "s_add_u32 s26, s26, 147456\n\t"
    "v_mfma_f32_16x16x32_bf16 v[232:235], a[132:135], v[188:191], v[232:235]\n\t"
    "s_add_u32 m0, s27, 102400\n\t"
    "v_cvt_pk_bf16_f32 v120, v168, v169\n\t"
    "v_cvt_pk_bf16_f32 v121, v170, v171\n\t"
    "v_mfma_f32_16x16x32_bf16 v[232:235], a[136:139], v[192:195], v[232:235]\n\t"
    "buffer_load_dwordx4 v224, s[76:79], s26 offen lds\n\t"
    "v_mfma_f32_16x16x32_bf16 v[232:235], a[140:143], v[196:199], v[232:235]\n\t"
    "s_add_u32 m0, s27, 103424\n\t"
    "v_cvt_pk_bf16_f32 v122, v172, v173\n\t"
    "v_cvt_pk_bf16_f32 v123, v174, v175\n\t"
    "v_mfma_f32_16x16x32_bf16 v[236:239], a[84:87], v[212:215], v[236:239]\n\t"
    "s_waitcnt vmcnt(4)\n\t"
    "s_barrier\n\t"
    "ds_read_b64_tr_b16 a[0:1], v24 offset:0\n\t"
    "ds_read_b64_tr_b16 a[2:3], v24 offset:1536\n\t"
    "v_mfma_f32_16x16x32_bf16 v[236:239], a[88:91], v[216:219], v[236:239]\n\t"
    "ds_read_b64_tr_b16 a[4:5], v24 offset:12288\n\t"
    "ds_read_b64_tr_b16 a[6:7], v24 offset:13824\n\t"
    "v_mfma_f32_16x16x32_bf16 v[236:239], a[92:95], v[220:223], v[236:239]\n\t"
    "buffer_load_dwordx4 v225, s[76:79], s26 offen lds\n\t"
    "v_mfma_f32_16x16x32_bf16 v[240:243], a[108:111], v[212:215], v[240:243]\n\t"
    "s_add_u32 m0, s27, 104448\n\t"
    "ds_read_b64_tr_b16 a[8:9], v24 offset:24576\n\t"
    "ds_read_b64_tr_b16 a[10:11], v24 offset:26112\n\t"
    "v_mfma_f32_16x16x32_bf16 v[240:243], a[112:115], v[216:219], v[240:243]\n\t"
    "ds_read_b64_tr_b16 a[24:25], v25 offset:0\n\t"
    "ds_read_b64_tr_b16 a[26:27], v25 offset:1536\n\t"
    "v_mfma_f32_16x16x32_bf16 v[240:243], a[116:119], v[220:223], v[240:243]\n\t"
    "ds_read_b64_tr_b16 a[28:29], v25 offset:12288\n\t"
    "ds_read_b64_tr_b16 a[30:31], v25 offset:13824\n\t"
    "v_mfma_f32_16x16x32_bf16 v[244:247], a[132:135], v[212:215], v[244:247]\n\t"
    "buffer_load_dwordx4 v226, s[76:79], s26 offen lds\n\t"
    "v_mfma_f32_16x16x32_bf16 v[244:247], a[136:139], v[216:219], v[244:247]\n\t"
    "s_mul_i32 s26, s73, 9216\n\t"
    "ds_read_b64_tr_b16 a[32:33], v25 offset:24576\n\t"
    "ds_read_b64_tr_b16 a[34:35], v25 offset:26112\n\t"
    "v_mfma_f32_16x16x32_bf16 v[244:247], a[140:143], v[220:223], v[244:247]\n\t"
    "s_waitcnt lgkmcnt(10)\n\t"
    "s_add_u32 s26, s26, 184320\n\t"
    "ds_read_b64_tr_b16 a[48:49], v26 offset:0\n\t"
    "ds_read_b64_tr_b16 a[50:51], v26 offset:1536\n\t"
    "v_mfma_f32_16x16x32_bf16 v[144:147], a[0:3], a[216:219], 0\n\t"
    "s_waitcnt lgkmcnt(10)\n\t"
    "s_add_u32 m0, s27, 114688\n\t"
    "ds_read_b64_tr_b16 a[52:53], v26 offset:12288\n\t"
    "ds_read_b64_tr_b16 a[54:55], v26 offset:13824\n\t"
    "v_mfma_f32_16x16x32_bf16 v[144:147], a[4:7], a[220:223], v[144:147]\n\t"
    "s_waitcnt lgkmcnt(10)\n\t"
    "buffer_load_dwordx4 v224, s[76:79], s26 offen lds\n\t"
    "v_mfma_f32_16x16x32_bf16 v[144:147], a[8:11], a[224:227], v[144:147]\n\t"
    "s_waitcnt lgkmcnt(8)\n\t"
    "s_add_u32 m0, s27, 115712\n\t"
    "ds_read_b64_tr_b16 a[56:57], v26 offset:24576\n\t"
    "ds_read_b64_tr_b16 a[58:59], v26 offset:26112\n\t"
    "v_mfma_f32_16x16x32_bf16 v[12:15], a[24:27], a[216:219], 0\n\t"
    "s_waitcnt lgkmcnt(8)\n\t"
    "v_cvt_pk_bf16_f32 v136, v232, v233\n\t"
    "v_cvt_pk_bf16_f32 v137, v234, v235\n\t"
    "v_mfma_f32_16x16x32_bf16 v[12:15], a[28:31], a[220:223], v[12:15]\n\t"
    "s_waitcnt lgkmcnt(6)\n\t"
    "v_cvt_pk_bf16_f32 v124, v236, v237\n\t"
    "v_cvt_pk_bf16_f32 v125, v238, v239\n\t"
    "v_mfma_f32_16x16x32_bf16 v[12:15], a[32:35], a[224:227], v[12:15]\n\t"
    "s_waitcnt lgkmcnt(4)\n\t"
    "buffer_load_dwordx4 v225, s[76:79], s26 offen lds\n\t"
    "v_mfma_f32_16x16x32_bf16 v[160:163], a[48:51], a[216:219], 0\n\t"
    "s_add_u32 m0, s27, 116736\n\t"
    "s_waitcnt lgkmcnt(2)\n\t"
    "v_mfma_f32_16x16x32_bf16 v[160:163], a[52:55], a[220:223], v[160:163]\n\t"
    "s_waitcnt lgkmcnt(0)\n\t"
    "v_mfma_f32_16x16x32_bf16 v[160:163], a[56:59], a[224:227], v[160:163]\n\t"
    "buffer_load_dwordx4 v226, s[76:79], s26 offen lds\n\t"
    "v_mfma_f32_16x16x32_bf16 v[148:151], a[0:3], a[240:243], 0\n\t"
    "s_add_u32 m0, s61, 24576\n\t"
    "v_mfma_f32_16x16x32_bf16 v[148:151], a[4:7], a[244:247], v[148:151]\n\t"
    "s_mul_i32 s26, s73, 9216\n\t"
    "v_mfma_f32_16x16x32_bf16 v[148:151], a[8:11], a[248:251], v[148:151]\n\t"
    "s_add_u32 s26, s26, 768\n\t"
    "v_mfma_f32_16x16x32_bf16 v[228:231], a[24:27], a[240:243], 0\n\t"
    "buffer_load_dwordx4 v10, s[40:43], s56 offen nt lds\n\t"
    "v_mfma_f32_16x16x32_bf16 v[228:231], a[28:31], a[244:247], v[228:231]\n\t"
    "s_add_u32 m0, s61, 28672\n\t"
    "v_mfma_f32_16x16x32_bf16 v[228:231], a[32:35], a[248:251], v[228:231]\n\t"
    "s_xor_b32 s61, s61, 0x8000\n\t"
    "v_mfma_f32_16x16x32_bf16 v[164:167], a[48:51], a[240:243], 0\n\t"
    "v_mfma_f32_16x16x32_bf16 v[164:167], a[52:55], a[244:247], v[164:167]\n\t"
    "buffer_load_dwordx4 v10, s[40:43], s57 offen nt lds\n\t"
    "v_mfma_f32_16x16x32_bf16 v[164:167], a[56:59], a[248:251], v[164:167]\n\t"
    "s_add_u32 m0, s27, 126976\n\t"
    "v_mfma_f32_16x16x32_bf16 v[152:155], a[0:3], v[176:179], 0\n\t"
    "s_mov_b64 s[56:57], s[70:71]\n\t"
    "v_mfma_f32_16x16x32_bf16 v[152:155], a[4:7], v[180:183], v[152:155]\n\t"
    "s_lshl_b32 s70, s72, 13\n\t"
    "v_mfma_f32_16x16x32_bf16 v[152:155], a[8:11], v[184:187], v[152:155]\n\t"
    "s_add_u32 s71, s70, 4096\n\t"
    "buffer_load_dwordx4 v224, s[76:79], s26 offen lds\n\t"
    "v_mfma_f32_16x16x32_bf16 v[232:235], a[24:27], v[176:179], 0\n\t"
    "s_add_u32 m0, s27, 128000\n\t"
    "v_mfma_f32_16x16x32_bf16 v[232:235], a[28:31], v[180:183], v[232:235]\n\t"
    "v_mfma_f32_16x16x32_bf16 v[232:235], a[32:35], v[184:187], v[232:235]\n\t"
    "v_mfma_f32_16x16x32_bf16 v[168:171], a[48:51], v[176:179], 0\n\t"
    "buffer_load_dwordx4 v225, s[76:79], s26 offen lds\n\t"
    "v_mfma_f32_16x16x32_bf16 v[168:171], a[52:55], v[180:183], v[168:171]\n\t"
    "s_add_u32 m0, s27, 129024\n\t"
    "v_mfma_f32_16x16x32_bf16 v[168:171], a[56:59], v[184:187], v[168:171]\n\t"
    "s_waitcnt vmcnt(4)\n\t"
    "s_barrier\n\t"
    "ds_read_b64_tr_b16 a[12:13], v24 offset:36864\n\t"
    "ds_read_b64_tr_b16 a[14:15], v24 offset:38400\n\t"
    "v_mfma_f32_16x16x32_bf16 v[156:159], a[0:3], v[200:203], 0\n\t"
    "ds_read_b64_tr_b16 a[16:17], v24 offset:49152\n\t"
    "ds_read_b64_tr_b16 a[18:19], v24 offset:50688\n\t"
    "v_mfma_f32_16x16x32_bf16 v[156:159], a[4:7], v[204:207], v[156:159]\n\t"
    "buffer_load_dwordx4 v226, s[76:79], s26 offen lds\n\t"
    "v_mfma_f32_16x16x32_bf16 v[156:159], a[8:11], v[208:211], v[156:159]\n\t"
    "s_mul_i32 s26, s73, 9216\n\t"
    "ds_read_b64_tr_b16 a[20:21], v24 offset:61440\n\t"
    "ds_read_b64_tr_b16 a[22:23], v24 offset:62976\n\t"
    "v_mfma_f32_16x16x32_bf16 v[236:239], a[24:27], v[200:203], 0\n\t"
    "s_add_u32 s26, s26, 37632\n\t"
    "ds_read_b64_tr_b16 a[36:37], v25 offset:36864\n\t"
    "ds_read_b64_tr_b16 a[38:39], v25 offset:38400\n\t"
    "v_mfma_f32_16x16x32_bf16 v[236:239], a[28:31], v[204:207], v[236:239]\n\t"
    "s_add_u32 m0, s27, 139264\n\t"
    "ds_read_b64_tr_b16 a[40:41], v25 offset:49152\n\t"
    "ds_read_b64_tr_b16 a[42:43], v25 offset:50688\n\t"
    "v_mfma_f32_16x16x32_bf16 v[236:239], a[32:35], v[208:211], v[236:239]\n\t"
    "buffer_load_dwordx4 v224, s[76:79], s26 offen lds\n\t"
    "v_mfma_f32_16x16x32_bf16 v[172:175], a[48:51], v[200:203], 0\n\t"
    "s_add_u32 m0, s27, 140288\n\t"
    "ds_read_b64_tr_b16 a[44:45], v25 offset:61440\n\t"
    "ds_read_b64_tr_b16 a[46:47], v25 offset:62976\n\t"
    "v_mfma_f32_16x16x32_bf16 v[172:175], a[52:55], v[204:207], v[172:175]\n\t"
    "ds_read_b64_tr_b16 a[60:61], v26 offset:36864\n\t"
    "ds_read_b64_tr_b16 a[62:63], v26 offset:38400\n\t"
    "v_mfma_f32_16x16x32_bf16 v[172:175], a[56:59], v[208:211], v[172:175]\n\t"
    "s_waitcnt lgkmcnt(12)\n\t"
    "ds_read_b64_tr_b16 a[64:65], v26 offset:49152\n\t"
    "ds_read_b64_tr_b16 a[66:67], v26 offset:50688\n\t"
    "v_mfma_f32_16x16x32_bf16 v[144:147], a[12:15], a[228:231], v[144:147]\n\t"
    "s_waitcnt lgkmcnt(12)\n\t"
    "buffer_load_dwordx4 v225, s[76:79], s26 offen lds\n\t"
    "v_mfma_f32_16x16x32_bf16 v[144:147], a[16:19], a[232:235], v[144:147]\n\t"
    "s_waitcnt lgkmcnt(10)\n\t"
    "s_add_u32 m0, s27, 141312\n\t"
    "ds_read_b64_tr_b16 a[68:69], v26 offset:61440\n\t"
    "ds_read_b64_tr_b16 a[70:71], v26 offset:62976\n\t"
    "v_mfma_f32_16x16x32_bf16 v[144:147], a[20:23], a[236:239], v[144:147]\n\t"
    "s_waitcnt lgkmcnt(10)\n\t"
    "v_mfma_f32_16x16x32_bf16 v[12:15], a[36:39], a[228:231], v[12:15]\n\t"
    "s_waitcnt lgkmcnt(8)\n\t"
    "v_mfma_f32_16x16x32_bf16 v[12:15], a[40:43], a[232:235], v[12:15]\n\t"
    "s_waitcnt lgkmcnt(6)\n\t"
    "buffer_load_dwordx4 v226, s[76:79], s26 offen lds\n\t"
    "v_mfma_f32_16x16x32_bf16 v[12:15], a[44:47], a[236:239], v[12:15]\n\t"
    "s_mul_i32 s26, s73, 9216\n\t"
    "s_waitcnt lgkmcnt(4)\n\t"
    "v_mfma_f32_16x16x32_bf16 v[160:163], a[60:63], a[228:231], v[160:163]\n\t"
    "s_add_u32 s26, s26, 74496\n\t"
    "s_waitcnt lgkmcnt(2)\n\t"
    "v_mfma_f32_16x16x32_bf16 v[160:163], a[64:67], a[232:235], v[160:163]\n\t"
    "s_waitcnt lgkmcnt(0)\n\t"
    "s_add_u32 m0, s27, 151552\n\t"
    "v_cvt_pk_bf16_f32 v144, v144, v145\n\t"
    "v_cvt_pk_bf16_f32 v145, v146, v147\n\t"
    "v_mfma_f32_16x16x32_bf16 v[160:163], a[68:71], a[236:239], v[160:163]\n\t"
    "buffer_load_dwordx4 v224, s[76:79], s26 offen lds\n\t"
    "v_mfma_f32_16x16x32_bf16 v[148:151], a[12:15], a[252:255], v[148:151]\n\t"
    "s_add_u32 m0, s27, 152576\n\t"
    "v_cvt_pk_bf16_f32 v146, v12, v13\n\t"
    "v_cvt_pk_bf16_f32 v147, v14, v15\n\t"
    "v_mfma_f32_16x16x32_bf16 v[148:151], a[16:19], v[16:19], v[148:151]\n\t"
    "v_mov_b32 v12, 0x3f803f80\n\t"
    "v_mov_b32 v13, 0x3f803f80\n\t"
    "v_mfma_f32_16x16x32_bf16 v[148:151], a[20:23], v[20:23], v[148:151]\n\t"
    "v_cvt_pk_bf16_f32 v160, v160, v161\n\t"
    "v_cvt_pk_bf16_f32 v161, v162, v163\n\t"
    "v_mfma_f32_16x16x32_bf16 v[228:231], a[36:39], a[252:255], v[228:231]\n\t"
    "buffer_load_dwordx4 v225, s[76:79], s26 offen lds\n\t"
    "v_mfma_f32_16x16x32_bf16 v[228:231], a[40:43], v[16:19], v[228:231]\n\t"
    "s_add_u32 m0, s27, 153600\n\t"
    "v_mov_b32 v14, 0x3f803f80\n\t"
    "v_mov_b32 v15, 0x3f803f80\n\t"
    "v_mfma_f32_16x16x32_bf16 v[228:231], a[44:47], v[20:23], v[228:231]\n\t"
    "v_cvt_pk_bf16_f32 v148, v148, v149\n\t"
    "v_cvt_pk_bf16_f32 v149, v150, v151\n\t"
    "v_mfma_f32_16x16x32_bf16 v[164:167], a[60:63], a[252:255], v[164:167]\n\t"
    "v_mfma_f32_16x16x32_bf16 v[164:167], a[64:67], v[16:19], v[164:167]\n\t"
    "buffer_load_dwordx4 v226, s[76:79], s26 offen lds\n\t"
    "v_mfma_f32_16x16x32_bf16 v[164:167], a[68:71], v[20:23], v[164:167]\n\t"
    "s_mul_i32 s26, s73, 9216\n\t"
    "v_mfma_f32_16x16x32_bf16 v[152:155], a[12:15], v[188:191], v[152:155]\n\t"
    "s_add_u32 s26, s26, 111360\n\t"
    "v_cvt_pk_bf16_f32 v150, v228, v229\n\t"
    "v_cvt_pk_bf16_f32 v151, v230, v231\n\t"
    "v_mfma_f32_16x16x32_bf16 v[152:155], a[16:19], v[192:195], v[152:155]\n\t"
    "s_add_u32 m0, s27, 53248\n\t"
    "v_mfma_f32_16x16x32_bf16 v[152:155], a[20:23], v[196:199], v[152:155]\n\t"
    "buffer_load_dwordx4 v224, s[76:79], s26 offen lds\n\t"
    "v_mfma_f32_16x16x32_bf16 v[232:235], a[36:39], v[188:191], v[232:235]\n\t"
    "s_add_u32 m0, s27, 54272\n\t"
    "v_cvt_pk_bf16_f32 v164, v164, v165\n\t"
    "v_cvt_pk_bf16_f32 v165, v166, v167\n\t"
    "v_mfma_f32_16x16x32_bf16 v[232:235], a[40:43], v[192:195], v[232:235]\n\t"
    "v_mfma_f32_16x16x32_bf16 v[232:235], a[44:47], v[196:199], v[232:235]\n\t"
    "v_mfma_f32_16x16x32_bf16 v[168:171], a[60:63], v[188:191], v[168:171]\n\t"
    "buffer_load_dwordx4 v225, s[76:79], s26 offen lds\n\t"
    "v_mfma_f32_16x16x32_bf16 v[168:171], a[64:67], v[192:195], v[168:171]\n\t"
    "s_add_u32 m0, s27, 55296\n\t"
    "v_cvt_pk_bf16_f32 v152, v152, v153\n\t"
    "v_cvt_pk_bf16_f32 v153, v154, v155\n\t"
    "v_mfma_f32_16x16x32_bf16 v[168:171], a[68:71], v[196:199], v[168:171]\n\t"
    "v_mfma_f32_16x16x32_bf16 v[156:159], a[12:15], v[212:215], v[156:159]\n\t"
    "v_cvt_pk_bf16_f32 v154, v232, v233\n\t"
    "v_cvt_pk_bf16_f32 v155, v234, v235\n\t"
    "v_mfma_f32_16x16x32_bf16 v[156:159], a[16:19], v[216:219], v[156:159]\n\t"
    "buffer_load_dwordx4 v226, s[76:79], s26 offen lds\n\t"
    "v_mfma_f32_16x16x32_bf16 v[156:159], a[20:23], v[220:223], v[156:159]\n\t"
    "s_mul_i32 s26, s73, 9216\n\t"
    "v_mfma_f32_16x16x32_bf16 v[236:239], a[36:39], v[212:215], v[236:239]\n\t"
    "s_add_u32 s26, s26, 148224\n\t"
    "v_cvt_pk_bf16_f32 v168, v168, v169\n\t"
    "v_cvt_pk_bf16_f32 v169, v170, v171\n\t"
    "v_mfma_f32_16x16x32_bf16 v[236:239], a[40:43], v[216:219], v[236:239]\n\t"
    "s_add_u32 m0, s27, 65536\n\t"
    "v_mfma_f32_16x16x32_bf16 v[236:239], a[44:47], v[220:223], v[236:239]\n\t"
    "buffer_load_dwordx4 v224, s[76:79], s26 offen lds\n\t"
    "v_mfma_f32_16x16x32_bf16 v[172:175], a[60:63], v[212:215], v[172:175]\n\t"
    "s_add_u32 m0, s27, 66560\n\t"
    "v_cvt_pk_bf16_f32 v156, v156, v157\n\t"
    "v_cvt_pk_bf16_f32 v157, v158, v159\n\t"
    "v_mfma_f32_16x16x32_bf16 v[172:175], a[64:67], v[216:219], v[172:175]\n\t"
    "s_waitcnt vmcnt(4)\n\t"
    "s_barrier\n\t"
    "ds_read_b64 v[162:163], v2 offset:0\n\t"
    "ds_read_b64 v[166:167], v2 offset:2048\n\t"
    "v_mfma_f32_16x16x32_bf16 v[172:175], a[68:71], v[220:223], v[172:175]\n\t"
    "ds_read_b64 v[170:171], v2 offset:4096\n\t"
    "v_cvt_pk_bf16_f32 v158, v236, v237\n\t"
    "v_mfma_f32_16x16x32_bf16 v[200:203], v[112:115], v[144:147], v[48:51]\n\t"
    "buffer_load_dwordx4 v225, s[76:79], s26 offen lds\n\t"
    "s_add_u32 m0, s27, 67584\n\t"
    "v_cvt_pk_bf16_f32 v159, v238, v239\n\t"
    "ds_read_b64_tr_b16 a[192:193], v254 offset:0\n\t"
    "ds_read_b64_tr_b16 a[194:195], v254 offset:1536\n\t"
    "v_cvt_pk_bf16_f32 v172, v172, v173\n\t"
    "v_cvt_pk_bf16_f32 v173, v174, v175\n\t"
    "ds_read_b64 v[174:175], v2 offset:6144\n\t"
    "v_xor_b32 v2, 0x20, v2\n\t"
    "ds_read_b64_tr_b16 a[196:197], v254 offset:12288\n\t"
    "ds_read_b64_tr_b16 a[198:199], v254 offset:13824\n\t"
    "s_waitcnt lgkmcnt(7)\n\t"
    "v_mfma_f32_16x16x32_bf16 v[200:203], v[128:131], v[160:163], v[200:203]\n\t"
    "buffer_load_dwordx4 v226, s[76:79], s26 offen lds\n\t"
    "v_mfma_f32_16x16x32_bf16 v[204:207], v[112:115], v[148:151], v[52:55]\n\t"
    "s_waitcnt lgkmcnt(6)\n\t"
    "s_mul_i32 s26, s73, 9216\n\t"
    "ds_read_b64 v[162:163], v2 offset:0\n\t"
    "ds_read_b64_tr_b16 a[200:201], v254 offset:24576\n\t"
    "v_mfma_f32_16x16x32_bf16 v[204:207], v[128:131], v[164:167], v[204:207]\n\t"
    "s_add_u32 s26, s26, 185088\n\t"
    "ds_read_b64 v[166:167], v2 offset:2048\n\t"
    "ds_read_b64_tr_b16 a[202:203], v254 offset:26112\n\t"
    "v_mfma_f32_16x16x32_bf16 v[208:211], v[112:115], v[152:155], v[56:59]\n\t"
    "s_waitcnt lgkmcnt(9)\n\t"
    "s_add_u32 m0, s27, 77824\n\t"
    "v_cvt_pk_bf16_f32 v140, v244, v245\n\t"
    "v_cvt_pk_bf16_f32 v126, v240, v241\n\t"
    "v_mfma_f32_16x16x32_bf16 v[208:211], v[128:131], v[168:171], v[208:211]\n\t"
    "buffer_load_dwordx4 v224, s[76:79], s26 offen lds\n\t"
    "v_mfma_f32_16x16x32_bf16 v[212:215], v[112:115], v[156:159], v[60:63]\n\t"
    "s_waitcnt lgkmcnt(6)\n\t"
    "s_add_u32 m0, s27, 78848\n\t"
    "ds_read_b64 v[170:171], v2 offset:4096\n\t"
    "v_cvt_pk_bf16_f32 v127, v242, v243\n\t"
    "v_mfma_f32_16x16x32_bf16 v[212:215], v[128:131], v[172:175], v[212:215]\n\t"
    "ds_read_b64 v[174:175], v2 offset:6144\n\t"
    "v_xor_b32 v2, 0x40, v2\n\t"
    "v_mfma_f32_16x16x32_bf16 v[216:219], v[116:119], v[144:147], v[64:67]\n\t"
    "s_waitcnt lgkmcnt(5)\n\t"
    "ds_read_b64_tr_b16 a[152:153], v252 offset:24576\n\t"
    "v_cvt_pk_bf16_f32 v141, v246, v247\n\t"
    "v_mfma_f32_16x16x32_bf16 v[216:219], v[132:135], v[160:163], v[216:219]\n\t"
    "buffer_load_dwordx4 v225, s[76:79], s26 offen lds\n\t"
    "v_mfma_f32_16x16x32_bf16 v[220:223], v[116:119], v[148:151], v[68:71]\n\t"
    "s_waitcnt lgkmcnt(4)\n\t"
    "s_add_u32 m0, s27, 79872\n\t"
    "ds_read_b64 v[162:163], v2 offset:0\n\t"
    "ds_read_b64_tr_b16 a[154:155], v252 offset:26112\n\t"
    "v_mfma_f32_16x16x32_bf16 v[220:223], v[132:135], v[164:167], v[220:223]\n\t"
    "ds_read_b64 v[166:167], v2 offset:2048\n\t"
    "ds_read_b64_tr_b16 a[148:149], v252 offset:12288\n\t"
    "ds_read_b64_tr_b16 a[150:151], v252 offset:13824\n\t"
    "ds_read_b64_tr_b16 a[144:145], v252 offset:0\n\t"
    "ds_read_b64_tr_b16 a[146:147], v252 offset:1536\n\t"
    "ds_read_b64_tr_b16 a[168:169], v253 offset:0\n\t"
    "ds_read_b64_tr_b16 a[170:171], v253 offset:1536\n\t"
    "buffer_load_dwordx4 v226, s[76:79], s26 offen lds\n\t"
    "v_mfma_f32_16x16x32_bf16 v[224:227], v[116:119], v[152:155], v[72:75]\n\t"
    "s_waitcnt lgkmcnt(11)\n\t"
    "s_add_u32 m0, s61, 0\n\t"
    "ds_read_b64_tr_b16 a[172:173], v253 offset:12288\n\t"
    "ds_read_b64_tr_b16 a[174:175], v253 offset:13824\n\t"
    "v_mfma_f32_16x16x32_bf16 v[224:227], v[132:135], v[168:171], v[224:227]\n\t"
    "ds_read_b64 v[170:171], v2 offset:4096\n\t"
    "ds_read_b64_tr_b16 a[176:177], v253 offset:24576\n\t"
    "v_mfma_f32_16x16x32_bf16 v[228:231], v[116:119], v[156:159], v[76:79]\n\t"
    "s_waitcnt lgkmcnt(14)\n\t"
    "ds_read_b64_tr_b16 a[178:179], v253 offset:26112\n\t"
    "v_max3_f32 v11, v200, v201, v202\n\t"
    "v_mfma_f32_16x16x32_bf16 v[228:231], v[132:135], v[172:175], v[228:231]\n\t"
    "buffer_load_dwordx4 v9, s[36:39], s48 offen nt lds\n\t"
    "v_mfma_f32_16x16x32_bf16 v[232:235], v[124:127], v[144:147], v[96:99]\n\t"
    "s_waitcnt lgkmcnt(13)\n\t"
    "s_add_u32 m0, s61, 8064\n\t"
    "ds_read_b64 v[174:175], v2 offset:6144\n\t"
    "v_xor_b32 v2, 0x20, v2\n\t"
    "v_mfma_f32_16x16x32_bf16 v[232:235], v[140:143], v[160:163], v[232:235]\n\t"
    "ds_read_b64 v[162:163], v2 offset:0\n\t"
    "v_max3_f32 v128, v204, v205, v206\n\t"
    "v_mfma_f32_16x16x32_bf16 v[236:239], v[124:127], v[148:151], v[100:103]\n\t"
    "s_waitcnt lgkmcnt(13)\n\t"
    "v_max3_f32 v129, v208, v209, v210\n\t"
    "v_max3_f32 v132, v212, v213, v214\n\t"
    "v_mfma_f32_16x16x32_bf16 v[236:239], v[140:143], v[164:167], v[236:239]\n\t"
    "buffer_load_dwordx4 v9, s[36:39], s48 offen offset:128 nt lds\n\t"
    "v_mfma_f32_16x16x32_bf16 v[240:243], v[124:127], v[152:155], v[104:107]\n\t"
    "s_waitcnt lgkmcnt(4)\n\t"
    "s_add_u32 m0, s61, 16128\n\t"
    "ds_read_b64 v[166:167], v2 offset:2048\n\t"
    "v_max_f32 v11, v11, v203\n\t"
    "v_mfma_f32_16x16x32_bf16 v[240:243], v[140:143], v[168:171], v[240:243]\n\t"
    "ds_read_b64 v[170:171], v2 offset:4096\n\t"
    "v_max_f32 v128, v128, v207\n\t"
    "v_mfma_f32_16x16x32_bf16 v[244:247], v[124:127], v[156:159], v[108:111]\n\t"
    "s_waitcnt lgkmcnt(3)\n\t"
    "v_max_f32 v129, v129, v211\n\t"
    "v_max_f32 v132, v132, v215\n\t"
    "v_mfma_f32_16x16x32_bf16 v[244:247], v[140:143], v[172:175], v[244:247]\n\t"
    "buffer_load_dwordx4 v9, s[36:39], s48 offen offset:256 nt lds\n\t"
    "v_mfma_f32_16x16x32_bf16 v[248:251], v[120:123], v[144:147], v[80:83]\n\t"
    "s_waitcnt lgkmcnt(2)\n\t"
    "s_add_u32 m0, s61, 4096\n\t"
    "ds_read_b128 v[144:147], v0 offset:0\n\t"
    "ds_read_b64 v[174:175], v2 offset:6144\n\t"
    "v_mfma_f32_16x16x32_bf16 v[248:251], v[136:139], v[160:163], v[248:251]\n\t"
    "ds_read_b128 v[160:163], v1 offset:0\n\t"
    "v_max3_f32 v11, v11, v216, v217\n\t"
    "v_mfma_f32_16x16x32_bf16 v[112:115], v[120:123], v[148:151], v[84:87]\n\t"
    "s_waitcnt lgkmcnt(4)\n\t"
    "ds_read_b128 v[148:151], v0 offset:8192\n\t"
    "v_max3_f32 v128, v128, v220, v221\n\t"
    "v_mfma_f32_16x16x32_bf16 v[112:115], v[136:139], v[164:167], v[112:115]\n\t"
    "buffer_load_dwordx4 v9, s[36:39], s49 offen nt lds\n\t"
    "v_mfma_f32_16x16x32_bf16 v[116:119], v[120:123], v[152:155], v[88:91]\n\t"
    "s_waitcnt lgkmcnt(4)\n\t"
    "s_add_u32 m0, s61, 12160\n\t"
    "ds_read_b128 v[164:167], v1 offset:8192\n\t"
    "ds_read_b128 v[152:155], v0 offset:16384\n\t"
    "v_mfma_f32_16x16x32_bf16 v[116:119], v[136:139], v[168:171], v[116:119]\n\t"
    "s_waitcnt vmcnt(4)\n\t"
    "s_barrier\n\t"
    "ds_read_b128 v[168:171], v1 offset:16384\n\t"
    "ds_read_b64_tr_b16 a[156:157], v24 offset:0\n\t"
    "v_mfma_f32_16x16x32_bf16 v[124:127], v[120:123], v[156:159], v[92:95]\n\t"
    "s_waitcnt lgkmcnt(6)\n\t"
    "ds_read_b64_tr_b16 a[158:159], v24 offset:1536\n\t"
    "ds_read_b64_tr_b16 a[160:161], v24 offset:12288\n\t"
    "ds_read_b64_tr_b16 a[162:163], v24 offset:13824\n\t"
    "v_mfma_f32_16x16x32_bf16 v[124:127], v[136:139], v[172:175], v[124:127]\n\t"
    "buffer_load_dwordx4 v9, s[36:39], s49 offen offset:128 nt lds\n\t"
    "v_mfma_f32_16x16x32_bf16 v[120:123], v[144:147], a[192:195], 0\n\t"
    "s_waitcnt lgkmcnt(8)\n\t"
    "ds_read_b64_tr_b16 a[164:165], v24 offset:24576\n\t"
    "ds_read_b64_tr_b16 a[166:167], v24 offset:26112\n\t"
    "v_mfma_f32_16x16x32_bf16 v[120:123], v[160:163], a[196:199], v[120:123]\n\t"
    "s_waitcnt lgkmcnt(9)\n\t"
    "ds_read_b64_tr_b16 a[180:181], v25 offset:0\n\t"
    "ds_read_b64_tr_b16 a[182:183], v25 offset:1536\n\t"
    "v_mfma_f32_16x16x32_bf16 v[120:123], v[148:151], a[200:203], v[120:123]\n\t"
    "ds_read_b64_tr_b16 a[184:185], v25 offset:12288\n\t"
    "ds_read_b64_tr_b16 a[186:187], v25 offset:13824\n\t"
    "ds_read_b64_tr_b16 a[204:205], v26 offset:0\n\t"
    "ds_read_b64_tr_b16 a[206:207], v26 offset:1536\n\t"
    "ds_read_b64_tr_b16 a[188:189], v25 offset:24576\n\t"
    "ds_read_b64_tr_b16 a[190:191], v25 offset:26112\n\t"
    "ds_read_b64_tr_b16 a[208:209], v26 offset:12288\n\t"
    "ds_read_b64_tr_b16 a[210:211], v26 offset:13824\n\t"
    "ds_read_b64_tr_b16 a[212:213], v26 offset:24576\n\t"
    "ds_read_b64_tr_b16 a[214:215], v26 offset:26112\n\t"
    "v_max3_f32 v129, v129, v224, v225\n\t"
    "v_max3_f32 v132, v132, v228, v229\n\t"
    "v_max3_f32 v11, v11, v218, v219\n\t"
    "v_max3_f32 v128, v128, v222, v223\n\t"
    "v_max3_f32 v129, v129, v226, v227\n\t"
    "v_max3_f32 v132, v132, v230, v231\n\t"
    "v_max3_f32 v11, v11, v232, v233\n\t"
    "v_max3_f32 v128, v128, v236, v237\n\t"
    "v_max3_f32 v129, v129, v240, v241\n\t"
    "v_max3_f32 v132, v132, v244, v245\n\t"
    "v_max3_f32 v11, v11, v234, v235\n\t"
    "v_max3_f32 v128, v128, v238, v239\n\t"
    "v_max3_f32 v129, v129, v242, v243\n\t"
    "v_max3_f32 v132, v132, v246, v247\n\t"
    "v_max3_f32 v11, v11, v248, v249\n\t"
    "v_max3_f32 v128, v128, v112, v113\n\t"
    "v_max3_f32 v129, v129, v116, v117\n\t"
    "v_max3_f32 v132, v132, v124, v125\n\t"
    "v_max3_f32 v11, v11, v250, v251\n\t"
    "v_max3_f32 v128, v128, v114, v115\n\t"
    "s_waitcnt lgkmcnt(6)\n\t"
    "v_mfma_f32_16x16x32_bf16 v[120:123], v[164:167], a[204:207], v[120:123]\n\t"
    "s_waitcnt lgkmcnt(2)\n\t"
    "v_max3_f32 v129, v129, v118, v119\n\t"
    "v_max3_f32 v132, v132, v126, v127\n\t"
    "v_mfma_f32_16x16x32_bf16 v[120:123], v[152:155], a[208:211], v[120:123]\n\t"
    "s_waitcnt lgkmcnt(0)\n\t"
    "v_permlane32_swap_b32 v11, v128\n\t"
    "v_mfma_f32_16x16x32_bf16 v[120:123], v[168:171], a[212:215], v[120:123]\n\t"
    "s_barrier\n\t"
    "v_permlane32_swap_b32 v129, v132\n\t"
    "v_mfma_f32_16x16x32_bf16 v[156:159], v[168:171], a[164:167], 0\n\t"
    "s_add_u32 m0, s61, 20224\n\t"
    "v_max_f32 v11, v11, v128\n\t"
    "v_max_f32 v129, v129, v132\n\t"
    "v_mfma_f32_16x16x32_bf16 v[156:159], v[152:155], a[160:163], v[156:159]\n\t"
    "buffer_load_dwordx4 v9, s[36:39], s49 offen offset:256 nt lds\n\t"
    "v_mfma_f32_16x16x32_bf16 v[156:159], v[164:167], a[156:159], v[156:159]\n\t"
    "s_add_u32 m0, s61, 24576\n\t"
    "v_cvt_pk_bf16_f32 v192, v120, v121\n\t"
    "v_cvt_pk_bf16_f32 v193, v122, v123\n\t"
    "v_mfma_f32_16x16x32_bf16 v[156:159], v[148:151], a[152:155], v[156:159]\n\t"
    "v_permlane16_swap_b32 v11, v129\n\t"
    "v_mfma_f32_16x16x32_bf16 v[156:159], v[160:163], a[148:151], v[156:159]\n\t"
    "v_max_f32 v11, v11, v129\n\t"
    "v_mul_f32 v11, 0xbe553b95, v11\n\t"
    "v_mfma_f32_16x16x32_bf16 v[156:159], v[144:147], a[144:147], v[156:159]\n\t"
    "s_mul_i32 s26, s73, 3072\n\t"
    "buffer_load_dwordx4 v10, s[40:43], s56 offen nt lds\n\t"
    "v_mfma_f32_16x16x32_bf16 v[252:255], v[144:147], a[168:171], 0\n\t"
    "s_add_u32 m0, s61, 28672\n\t"
    "ds_read_b128 v[144:147], v0 offset:2048\n\t"
    "v_mov_b32 v129, v11\n\t"
    "v_mfma_f32_16x16x32_bf16 v[252:255], v[160:163], a[172:175], v[252:255]\n\t"
    "s_add_u32 s26, s26, 0\n\t"
    "ds_read_b128 v[160:163], v1 offset:2048\n\t"
    "v_cvt_pk_bf16_f32 v176, v156, v157\n\t"
    "v_mfma_f32_16x16x32_bf16 v[252:255], v[148:151], a[176:179], v[252:255]\n\t"
    "ds_read_b128 v[148:151], v0 offset:10240\n\t"
    "v_cvt_pk_bf16_f32 v177, v158, v159\n\t"
    "v_mfma_f32_16x16x32_bf16 v[252:255], v[164:167], a[180:183], v[252:255]\n\t"
    "buffer_load_dwordx4 v10, s[40:43], s57 offen nt lds\n\t"
    "v_mfma_f32_16x16x32_bf16 v[252:255], v[152:155], a[184:187], v[252:255]\n\t"
    "s_add_u32 m0, s27, 90112\n\t"
    "ds_read_b128 v[164:167], v1 offset:10240\n\t"
    "ds_read_b128 v[152:155], v0 offset:18432\n\t"
    "v_mfma_f32_16x16x32_bf16 v[252:255], v[168:171], a[188:191], v[252:255]\n\t"
    "ds_read_b128 v[168:171], v1 offset:18432\n\t"
    "v_xor_b32 v2, 0x8040, v2\n\t"
    "v_permlane16_swap_b32 v11, v129\n\t"
    "v_mov_b32 v128, v11\n\t"
    "v_mov_b32 v132, v129\n\t"
    "s_waitcnt lgkmcnt(5)\n\t"
    "v_mfma_f32_16x16x32_bf16 v[120:123], v[144:147], a[192:195], 0\n\t"
    "s_waitcnt lgkmcnt(4)\n\t"
    "buffer_load_dwordx4 v40, s[28:31], s26 offen lds\n\t"
    "v_mfma_f32_16x16x32_bf16 v[120:123], v[160:163], a[196:199], v[120:123]\n\t"
    "s_waitcnt lgkmcnt(3)\n\t"
    "s_add_u32 m0, s27, 91136\n\t"
    "v_cvt_pk_bf16_f32 v184, v252, v253\n\t"
    "v_cvt_pk_bf16_f32 v185, v254, v255\n\t"
    "v_mfma_f32_16x16x32_bf16 v[120:123], v[148:151], a[200:203], v[120:123]\n\t"
    "v_permlane32_swap_b32 v11, v128\n\t"
    "v_fma_f32 v200, v200, s62, v11\n\t"
    "v_fma_f32 v201, v201, s62, v11\n\t"
    "v_exp_f32 v200, v200\n\t"
    "s_waitcnt lgkmcnt(2)\n\t"
    "v_mfma_f32_16x16x32_bf16 v[120:123], v[164:167], a[204:207], v[120:123]\n\t"
    "s_waitcnt lgkmcnt(1)\n\t"
    "buffer_load_dwordx4 v41, s[28:31], s26 offen lds\n\t"
    "v_mfma_f32_16x16x32_bf16 v[120:123], v[152:155], a[208:211], v[120:123]\n\t"
    "s_add_u32 m0, s27, 92160\n\t"
    "v_exp_f32 v201, v201\n\t"
    "s_waitcnt lgkmcnt(0)\n\t"
    "v_mfma_f32_16x16x32_bf16 v[120:123], v[168:171], a[212:215], v[120:123]\n\t"
    "v_fma_f32 v202, v202, s62, v11\n\t"
    "v_fma_f32 v203, v203, s62, v11\n\t"
    "v_mfma_f32_16x16x32_bf16 v[156:159], v[168:171], a[164:167], 0\n\t"
    "v_exp_f32 v202, v202\n\t"
    "v_mfma_f32_16x16x32_bf16 v[156:159], v[152:155], a[160:163], v[156:159]\n\t"
    "buffer_load_dwordx4 v42, s[28:31], s26 offen lds\n\t"
    "v_mfma_f32_16x16x32_bf16 v[156:159], v[164:167], a[156:159], v[156:159]\n\t"
    "s_mul_i32 s26, s73, 3072\n\t"
    "v_cvt_pk_bf16_f32 v194, v120, v121\n\t"
    "v_cvt_pk_bf16_f32 v195, v122, v123\n\t"
    "v_mfma_f32_16x16x32_bf16 v[156:159], v[148:151], a[152:155], v[156:159]\n\t"
    "s_add_u32 s26, s26, 12288\n\t"
    "v_exp_f32 v203, v203\n\t"
    "v_mfma_f32_16x16x32_bf16 v[156:159], v[160:163], a[148:151], v[156:159]\n\t"
    "s_add_u32 m0, s27, 102400\n\t"
    "v_fma_f32 v216, v216, s62, v11\n\t"
    "v_fma_f32 v217, v217, s62, v11\n\t"
    "v_mfma_f32_16x16x32_bf16 v[156:159], v[144:147], a[144:147], v[156:159]\n\t"
    "buffer_load_dwordx4 v40, s[28:31], s26 offen lds\n\t"
    "v_mfma_f32_16x16x32_bf16 v[252:255], v[144:147], a[168:171], 0\n\t"
    "ds_read_b128 v[144:147], v0 offset:4096\n\t"
    "v_fma_f32 v218, v218, s62, v11\n\t"
    "v_mfma_f32_16x16x32_bf16 v[252:255], v[160:163], a[172:175], v[252:255]\n\t"
    "ds_read_b128 v[160:163], v1 offset:4096\n\t"
    "v_fma_f32 v219, v219, s62, v11\n\t"
    "v_mfma_f32_16x16x32_bf16 v[252:255], v[148:151], a[176:179], v[252:255]\n\t"
    "ds_read_b128 v[148:151], v0 offset:12288\n\t"
    "v_cvt_pk_bf16_f32 v178, v156, v157\n\t"
    "v_mfma_f32_16x16x32_bf16 v[252:255], v[164:167], a[180:183], v[252:255]\n\t"
    "ds_read_b128 v[164:167], v1 offset:12288\n\t"
    "v_cvt_pk_bf16_f32 v179, v158, v159\n\t"
    "v_mfma_f32_16x16x32_bf16 v[252:255], v[152:155], a[184:187], v[252:255]\n\t"
    "ds_read_b128 v[152:155], v0 offset:20480\n\t"
    "v_cvt_pk_bf16_f32 v200, v200, v201\n\t"
    "v_mfma_f32_16x16x32_bf16 v[252:255], v[168:171], a[188:191], v[252:255]\n\t"
    "ds_read_b128 v[168:171], v1 offset:20480\n\t"
    "v_cvt_pk_bf16_f32 v201, v202, v203\n\t"
    "v_exp_f32 v216, v216\n\t"
    "v_exp_f32 v217, v217\n\t"
    "v_fma_f32 v248, v248, s62, v11\n\t"
    "s_waitcnt lgkmcnt(5)\n\t"
    "v_mfma_f32_16x16x32_bf16 v[120:123], v[144:147], a[192:195], 0\n\t"
    "s_waitcnt lgkmcnt(4)\n\t"
    "v_exp_f32 v218, v218\n\t"
    "v_mfma_f32_16x16x32_bf16 v[120:123], v[160:163], a[196:199], v[120:123]\n\t"
    "s_waitcnt lgkmcnt(3)\n\t"
    "v_cvt_pk_bf16_f32 v186, v252, v253\n\t"
    "v_cvt_pk_bf16_f32 v187, v254, v255\n\t"
    "v_mfma_f32_16x16x32_bf16 v[120:123], v[148:151], a[200:203], v[120:123]\n\t"
    "s_waitcnt lgkmcnt(2)\n\t"
    "v_exp_f32 v219, v219\n\t"
    "v_mfma_f32_16x16x32_bf16 v[120:123], v[164:167], a[204:207], v[120:123]\n\t"
    "s_waitcnt lgkmcnt(1)\n\t"
    "v_fma_f32 v249, v249, s62, v11\n\t"
    "v_cvt_pk_bf16_f32 v202, v216, v217\n\t"
    "v_mfma_f32_16x16x32_bf16 v[120:123], v[152:155], a[208:211], v[120:123]\n\t"
    "s_waitcnt lgkmcnt(0)\n\t"
    "v_cvt_pk_bf16_f32 v203, v218, v219\n\t"
    "v_fma_f32 v250, v250, s62, v11\n\t"
    "v_mfma_f32_16x16x32_bf16 v[120:123], v[168:171], a[212:215], v[120:123]\n\t"
    "v_exp_f32 v248, v248\n\t"
    "v_mfma_f32_16x16x32_bf16 v[156:159], v[168:171], a[164:167], 0\n\t"
    "v_exp_f32 v249, v249\n\t"
    "v_mfma_f32_16x16x32_bf16 v[156:159], v[152:155], a[160:163], v[156:159]\n\t"
    "v_fma_f32 v251, v251, s62, v11\n\t"
    "v_fma_f32 v232, v232, s62, v11\n\t"
    "v_mfma_f32_16x16x32_bf16 v[156:159], v[164:167], a[156:159], v[156:159]\n\t"
    "v_exp_f32 v250, v250\n\t"
    "v_mfma_f32_16x16x32_bf16 v[156:159], v[148:151], a[152:155], v[156:159]\n\t"
    "v_cvt_pk_bf16_f32 v196, v120, v121\n\t"
    "v_cvt_pk_bf16_f32 v197, v122, v123\n\t"
    "v_mfma_f32_16x16x32_bf16 v[156:159], v[160:163], a[148:151], v[156:159]\n\t"
    "v_exp_f32 v251, v251\n\t"
    "v_mfma_f32_16x16x32_bf16 v[156:159], v[144:147], a[144:147], v[156:159]\n\t"
    "v_fma_f32 v233, v233, s62, v11\n\t"
    "v_fma_f32 v234, v234, s62, v11\n\t"
    "v_mfma_f32_16x16x32_bf16 v[252:255], v[144:147], a[168:171], 0\n\t"
    "ds_read_b128 v[144:147], v0 offset:6144\n\t"
    "v_fma_f32 v235, v235, s62, v11\n\t"
    "v_mfma_f32_16x16x32_bf16 v[252:255], v[160:163], a[172:175], v[252:255]\n\t"
    "ds_read_b128 v[160:163], v1 offset:6144\n\t"
    "v_cvt_pk_bf16_f32 v248, v248, v249\n\t"
    "v_mfma_f32_16x16x32_bf16 v[252:255], v[148:151], a[176:179], v[252:255]\n\t"
    "ds_read_b128 v[148:151], v0 offset:14336\n\t"
    "v_cvt_pk_bf16_f32 v180, v156, v157\n\t"
    "v_mfma_f32_16x16x32_bf16 v[252:255], v[164:167], a[180:183], v[252:255]\n\t"
    "ds_read_b128 v[164:167], v1 offset:14336\n\t"
    "v_cvt_pk_bf16_f32 v181, v158, v159\n\t"
    "v_mfma_f32_16x16x32_bf16 v[252:255], v[152:155], a[184:187], v[252:255]\n\t"
    "ds_read_b128 v[152:155], v0 offset:22528\n\t"
    "v_xor_b32 v0, 0x8000, v0\n\t"
    "v_mfma_f32_16x16x32_bf16 v[252:255], v[168:171], a[188:191], v[252:255]\n\t"
    "ds_read_b128 v[168:171], v1 offset:22528\n\t"
    "v_xor_b32 v1, 0x8000, v1\n\t"
    "v_exp_f32 v232, v232\n\t"
    "v_exp_f32 v233, v233\n\t"
    "v_cvt_pk_bf16_f32 v249, v250, v251\n\t"
    "s_waitcnt lgkmcnt(5)\n\t"
    "v_mfma_f32_16x16x32_bf16 v[120:123], v[144:147], a[192:195], 0\n\t"
    "s_waitcnt lgkmcnt(4)\n\t"
    "v_exp_f32 v234, v234\n\t"
    "v_mfma_f32_16x16x32_bf16 v[120:123], v[160:163], a[196:199], v[120:123]\n\t"
    "s_waitcnt lgkmcnt(3)\n\t"
    "v_cvt_pk_bf16_f32 v188, v252, v253\n\t"
    "v_cvt_pk_bf16_f32 v189, v254, v255\n\t"
    "v_mfma_f32_16x16x32_bf16 v[120:123], v[148:151], a[200:203], v[120:123]\n\t"
    "s_waitcnt lgkmcnt(2)\n\t"
    "v_exp_f32 v235, v235\n\t"
    "v_mfma_f32_16x16x32_bf16 v[120:123], v[164:167], a[204:207], v[120:123]\n\t"
    "s_waitcnt lgkmcnt(1)\n\t"
    "v_cvt_pk_bf16_f32 v250, v232, v233\n\t"
    "v_cvt_pk_bf16_f32 v251, v234, v235\n\t"
    "v_mfma_f32_16x16x32_bf16 v[120:123], v[152:155], a[208:211], v[120:123]\n\t"
    "s_waitcnt lgkmcnt(0)\n\t"
    "v_fma_f32 v204, v204, s62, v128\n\t"
    "v_fma_f32 v205, v205, s62, v128\n\t"
    "v_mfma_f32_16x16x32_bf16 v[120:123], v[168:171], a[212:215], v[120:123]\n\t"
    "v_exp_f32 v204, v204\n\t"
    "v_mfma_f32_16x16x32_bf16 v[156:159], v[168:171], a[164:167], 0\n\t"
    "v_exp_f32 v205, v205\n\t"
    "v_mfma_f32_16x16x32_bf16 v[156:159], v[152:155], a[160:163], v[156:159]\n\t"
    "v_fma_f32 v206, v206, s62, v128\n\t"
    "v_fma_f32 v207, v207, s62, v128\n\t"
    "v_mfma_f32_16x16x32_bf16 v[156:159], v[164:167], a[156:159], v[156:159]\n\t"
    "v_exp_f32 v206, v206\n\t"
    "v_mfma_f32_16x16x32_bf16 v[156:159], v[148:151], a[152:155], v[156:159]\n\t"
    "v_cvt_pk_bf16_f32 v198, v120, v121\n\t"
    "v_cvt_pk_bf16_f32 v199, v122, v123\n\t"
    "v_mfma_f32_16x16x32_bf16 v[156:159], v[160:163], a[148:151], v[156:159]\n\t"
    "v_exp_f32 v207, v207\n\t"
    "v_mfma_f32_16x16x32_bf16 v[156:159], v[144:147], a[144:147], v[156:159]\n\t"
    "v_fma_f32 v220, v220, s62, v128\n\t"
    "v_fma_f32 v221, v221, s62, v128\n\t"
    "v_mfma_f32_16x16x32_bf16 v[252:255], v[144:147], a[168:171], 0\n\t"
    "v_fma_f32 v222, v222, s62, v128\n\t"
    "v_fma_f32 v223, v223, s62, v128\n\t"
    "v_mfma_f32_16x16x32_bf16 v[252:255], v[160:163], a[172:175], v[252:255]\n\t"
    "v_cvt_pk_bf16_f32 v204, v204, v205\n\t"
    "v_cvt_pk_bf16_f32 v205, v206, v207\n\t"
    "v_mfma_f32_16x16x32_bf16 v[252:255], v[148:151], a[176:179], v[252:255]\n\t"
    "v_cvt_pk_bf16_f32 v182, v156, v157\n\t"
    "v_cvt_pk_bf16_f32 v183, v158, v159\n\t"
    "v_mfma_f32_16x16x32_bf16 v[252:255], v[164:167], a[180:183], v[252:255]\n\t"
    "v_exp_f32 v220, v220\n\t"
    "v_mfma_f32_16x16x32_bf16 v[252:255], v[152:155], a[184:187], v[252:255]\n\t"
    "v_exp_f32 v221, v221\n\t"
    "v_mfma_f32_16x16x32_bf16 v[252:255], v[168:171], a[188:191], v[252:255]\n\t"
    "v_exp_f32 v222, v222\n\t"
    "v_mfma_f32_16x16x32_bf16 v[216:219], v[12:15], v[200:203], 0\n\t"
    "v_exp_f32 v223, v223\n\t"
    "v_mfma_f32_16x16x32_bf16 v[216:219], v[12:15], v[248:251], v[216:219]\n\t"
    "v_fma_f32 v112, v112, s62, v128\n\t"
    "v_fma_f32 v113, v113, s62, v128\n\t"
    "v_mfma_f32_16x16x32_bf16 v[232:235], v[176:179], v[200:203], 0\n\t"
    "v_cvt_pk_bf16_f32 v206, v220, v221\n\t"
    "v_cvt_pk_bf16_f32 v190, v252, v253\n\t"
    "v_mfma_f32_16x16x32_bf16 v[232:235], v[180:183], v[248:251], v[232:235]\n\t"
    "v_cvt_pk_bf16_f32 v191, v254, v255\n\t"
    "v_cvt_pk_bf16_f32 v207, v222, v223\n\t"
    "v_mfma_f32_16x16x32_bf16 v[172:175], v[184:187], v[200:203], 0\n\t"
    "v_rcp_f32 v136, v216\n\t"
    "v_mfma_f32_16x16x32_bf16 v[172:175], v[188:191], v[248:251], v[172:175]\n\t"
    "v_exp_f32 v112, v112\n\t"
    "v_mfma_f32_16x16x32_bf16 v[216:219], v[192:195], v[200:203], 0\n\t"
    "v_exp_f32 v113, v113\n\t"
    "v_mfma_f32_16x16x32_bf16 v[216:219], v[196:199], v[248:251], v[216:219]\n\t"
    "v_fma_f32 v114, v114, s62, v128\n\t"
    "v_fma_f32 v115, v115, s62, v128\n\t"
    "v_mfma_f32_16x16x32_bf16 v[220:223], v[12:15], v[204:207], 0\n\t"
    "v_exp_f32 v114, v114\n\t"
    "v_exp_f32 v115, v115\n\t"
    "v_fma_f32 v236, v236, s62, v128\n\t"
    "v_fma_f32 v237, v237, s62, v128\n\t"
    "v_fma_f32 v238, v238, s62, v128\n\t"
    "v_fma_f32 v239, v239, s62, v128\n\t"
    "v_cvt_pk_bf16_f32 v112, v112, v113\n\t"
    "v_exp_f32 v236, v236\n\t"
    "v_exp_f32 v237, v237\n\t"
    "v_exp_f32 v238, v238\n\t"
    "v_exp_f32 v239, v239\n\t"
    "v_cvt_pk_bf16_f32 v113, v114, v115\n\t"
    "v_cvt_pk_bf16_f32 v114, v236, v237\n\t"
    "v_cvt_pk_bf16_f32 v115, v238, v239\n\t"
    "v_permlane32_swap_b32 v129, v132\n\t"
    "s_nop 0\n\t"
    "v_mfma_f32_16x16x32_bf16 v[220:223], v[12:15], v[112:115], v[220:223]\n\t"
    "v_fma_f32 v208, v208, s62, v129\n\t"
    "v_fma_f32 v209, v209, s62, v129\n\t"
    "v_mfma_f32_16x16x32_bf16 v[236:239], v[176:179], v[204:207], 0\n\t"
    "v_exp_f32 v208, v208\n\t"
    "v_mfma_f32_16x16x32_bf16 v[236:239], v[180:183], v[112:115], v[236:239]\n\t"
    "v_exp_f32 v209, v209\n\t"
    "v_mfma_f32_16x16x32_bf16 v[200:203], v[184:187], v[204:207], 0\n\t"
    "v_fma_f32 v210, v210, s62, v129\n\t"
    "v_fma_f32 v211, v211, s62, v129\n\t"
    "v_mfma_f32_16x16x32_bf16 v[200:203], v[188:191], v[112:115], v[200:203]\n\t"
    "v_rcp_f32 v133, v220\n\t"
    "v_mfma_f32_16x16x32_bf16 v[248:251], v[192:195], v[204:207], 0\n\t"
    "v_exp_f32 v210, v210\n\t"
    "v_mfma_f32_16x16x32_bf16 v[248:251], v[196:199], v[112:115], v[248:251]\n\t"
    "v_exp_f32 v211, v211\n\t"
    "v_fma_f32 v224, v224, s62, v129\n\t"
    "v_fma_f32 v225, v225, s62, v129\n\t"
    "v_fma_f32 v226, v226, s62, v129\n\t"
    "v_fma_f32 v227, v227, s62, v129\n\t"
    "v_cvt_pk_bf16_f32 v208, v208, v209\n\t"
    "v_exp_f32 v224, v224\n\t"
    "v_exp_f32 v225, v225\n\t"
    "v_exp_f32 v226, v226\n\t"
    "v_exp_f32 v227, v227\n\t"
    "v_cvt_pk_bf16_f32 v209, v210, v211\n\t"
    "v_cvt_pk_bf16_f32 v210, v224, v225\n\t"
    "v_cvt_pk_bf16_f32 v211, v226, v227\n\t"
    "v_fma_f32 v116, v116, s62, v129\n\t"
    "v_fma_f32 v117, v117, s62, v129\n\t"
    "v_mfma_f32_16x16x32_bf16 v[224:227], v[12:15], v[208:211], 0\n\t"
    "v_exp_f32 v116, v116\n\t"
    "v_exp_f32 v117, v117\n\t"
    "v_fma_f32 v118, v118, s62, v129\n\t"
    "v_fma_f32 v119, v119, s62, v129\n\t"
    "v_exp_f32 v118, v118\n\t"
    "v_exp_f32 v119, v119\n\t"
    "v_fma_f32 v240, v240, s62, v129\n\t"
    "v_fma_f32 v241, v241, s62, v129\n\t"
    "v_fma_f32 v242, v242, s62, v129\n\t"
    "v_fma_f32 v243, v243, s62, v129\n\t"
    "v_cvt_pk_bf16_f32 v116, v116, v117\n\t"
    "v_exp_f32 v240, v240\n\t"
    "v_exp_f32 v241, v241\n\t"
    "v_exp_f32 v242, v242\n\t"
    "v_exp_f32 v243, v243\n\t"
    "v_cvt_pk_bf16_f32 v117, v118, v119\n\t"
    "v_cvt_pk_bf16_f32 v118, v240, v241\n\t"
    "v_cvt_pk_bf16_f32 v119, v242, v243\n\t"
    "v_fma_f32 v212, v212, s62, v132\n\t"
    "v_fma_f32 v213, v213, s62, v132\n\t"
    "v_mfma_f32_16x16x32_bf16 v[224:227], v[12:15], v[116:119], v[224:227]\n\t"
    "v_exp_f32 v212, v212\n\t"
    "v_mfma_f32_16x16x32_bf16 v[240:243], v[176:179], v[208:211], 0\n\t"
    "v_exp_f32 v213, v213\n\t"
    "v_mfma_f32_16x16x32_bf16 v[240:243], v[180:183], v[116:119], v[240:243]\n\t"
    "v_fma_f32 v214, v214, s62, v132\n\t"
    "v_fma_f32 v215, v215, s62, v132\n\t"
    "v_mfma_f32_16x16x32_bf16 v[204:207], v[184:187], v[208:211], 0\n\t"
    "v_exp_f32 v214, v214\n\t"
    "v_mfma_f32_16x16x32_bf16 v[204:207], v[188:191], v[116:119], v[204:207]\n\t"
    "v_rcp_f32 v140, v224\n\t"
    "v_mfma_f32_16x16x32_bf16 v[112:115], v[192:195], v[208:211], 0\n\t"
    "v_exp_f32 v215, v215\n\t"
    "v_mfma_f32_16x16x32_bf16 v[112:115], v[196:199], v[116:119], v[112:115]\n\t"
    "s_waitcnt vmcnt(4)\n\t"
    "s_barrier\n\t"
    "ds_read_b128 v[148:151], v0 offset:0\n\t"
    "ds_read_b128 v[152:155], v1 offset:0\n\t"
    "v_fma_f32 v228, v228, s62, v132\n\t"
    "v_fma_f32 v229, v229, s62, v132\n\t"
    "v_fma_f32 v230, v230, s62, v132\n\t"
    "v_fma_f32 v231, v231, s62, v132\n\t"
    "v_cvt_pk_bf16_f32 v212, v212, v213\n\t"
    "v_exp_f32 v228, v228\n\t"
    "v_exp_f32 v229, v229\n\t"
    "v_exp_f32 v230, v230\n\t"
    "v_exp_f32 v231, v231\n\t"
    "v_cvt_pk_bf16_f32 v213, v214, v215\n\t"
    "v_cvt_pk_bf16_f32 v214, v228, v229\n\t"
    "v_cvt_pk_bf16_f32 v215, v230, v231\n\t"
    "ds_read_b128 v[156:159], v0 offset:8192\n\t"
    "s_nop 0\n\t"
    "v_mfma_f32_16x16x32_bf16 v[228:231], v[12:15], v[212:215], 0\n\t"
    "ds_read_b128 v[164:167], v1 offset:8192\n\t"
    "ds_read_b128 v[168:171], v0 offset:16384\n\t"
    "v_fma_f32 v124, v124, s62, v132\n\t"
    "v_fma_f32 v125, v125, s62, v132\n\t"
    "v_exp_f32 v124, v124\n\t"
    "v_exp_f32 v125, v125\n\t"
    "v_fma_f32 v126, v126, s62, v132\n\t"
    "v_fma_f32 v127, v127, s62, v132\n\t"
    "v_exp_f32 v126, v126\n\t"
    "v_exp_f32 v127, v127\n\t"
    "v_fma_f32 v244, v244, s62, v132\n\t"
    "v_fma_f32 v245, v245, s62, v132\n\t"
    "v_fma_f32 v246, v246, s62, v132\n\t"
    "v_fma_f32 v247, v247, s62, v132\n\t"
    "v_cvt_pk_bf16_f32 v124, v124, v125\n\t"
    "v_exp_f32 v244, v244\n\t"
    "v_exp_f32 v245, v245\n\t"
    "v_exp_f32 v246, v246\n\t"
    "v_exp_f32 v247, v247\n\t"
    "v_cvt_pk_bf16_f32 v125, v126, v127\n\t"
    "v_cvt_pk_bf16_f32 v126, v244, v245\n\t"
    "v_cvt_pk_bf16_f32 v127, v246, v247\n\t"
    "ds_read_b128 v[224:227], v1 offset:16384\n\t"
    "s_nop 0\n\t"
    "v_mfma_f32_16x16x32_bf16 v[228:231], v[12:15], v[124:127], v[228:231]\n\t"
    "v_mfma_f32_16x16x32_bf16 v[244:247], v[176:179], v[212:215], 0\n\t"
    "v_mfma_f32_16x16x32_bf16 v[244:247], v[180:183], v[124:127], v[244:247]\n\t"
    "v_mfma_f32_16x16x32_bf16 v[208:211], v[184:187], v[212:215], 0\n\t"
    "v_mfma_f32_16x16x32_bf16 v[208:211], v[188:191], v[124:127], v[208:211]\n\t"
    "v_mfma_f32_16x16x32_bf16 v[116:119], v[192:195], v[212:215], 0\n\t"
    "v_mfma_f32_16x16x32_bf16 v[116:119], v[196:199], v[124:127], v[116:119]\n\t"
    "s_nop 1\n\t"
    "v_rcp_f32 v141, v228\n\t"
    "s_add_u32 m0, s27, 103424\n\t"
    "s_waitcnt lgkmcnt(5)\n\t"
    "v_mfma_f32_16x16x32_bf16 v[220:223], a[0:3], v[148:151], 0\n\t"
    "s_mov_b64 s[54:55], s[52:53]\n\t"
    "buffer_load_dwordx4 v41, s[28:31], s26 offen lds\n\t"
    "s_waitcnt lgkmcnt(4)\n\t"
    "v_mfma_f32_16x16x32_bf16 v[220:223], a[4:7], v[152:155], v[220:223]\n\t"
    "s_waitcnt lgkmcnt(3)\n\t"
    "s_add_u32 m0, s27, 104448\n\t"
    "ds_read_b128 v[176:179], v0 offset:2048\n\t"
    "ds_read_b128 v[180:183], v1 offset:2048\n\t"
    "v_mfma_f32_16x16x32_bf16 v[220:223], a[8:11], v[156:159], v[220:223]\n\t"
    "s_waitcnt lgkmcnt(4)\n\t"
    "s_mov_b64 s[52:53], s[50:51]\n\t"
    "ds_read_b128 v[184:187], v0 offset:10240\n\t"
    "ds_read_b128 v[188:191], v1 offset:10240\n\t"
    "v_mfma_f32_16x16x32_bf16 v[220:223], a[12:15], v[164:167], v[220:223]\n\t"
    "s_waitcnt lgkmcnt(5)\n\t"
    "s_mov_b64 s[50:51], s[48:49]\n\t"
    "ds_read_b128 v[192:195], v0 offset:18432\n\t"
    "ds_read_b128 v[196:199], v1 offset:18432\n\t"
    "v_mfma_f32_16x16x32_bf16 v[220:223], a[16:19], v[168:171], v[220:223]\n\t"
    "s_xor_b32 s61, s61, 0x8000\n\t"
    "buffer_load_dwordx4 v42, s[28:31], s26 offen lds\n\t"
    "s_waitcnt lgkmcnt(6)\n\t"
    "v_mfma_f32_16x16x32_bf16 v[220:223], a[20:23], v[224:227], v[220:223]\n\t"
    "s_mov_b64 s[48:49], s[68:69]\n\t"
    "v_mul_f32 v112, v140, v112\n\t"
    "v_mul_f32 v113, v140, v113\n\t"
    "v_mfma_f32_16x16x32_bf16 v[120:123], a[44:47], v[224:227], 0\n\t"
    "s_mul_i32 s26, s73, 3072\n\t"
    "v_mul_f32 v114, v140, v114\n\t"
    "v_mul_f32 v115, v140, v115\n\t"
    "v_mfma_f32_16x16x32_bf16 v[120:123], a[40:43], v[168:171], v[120:123]\n\t"
    "s_add_u32 s26, s26, 24576\n\t"
    "v_cvt_pk_bf16_f32 v144, v220, v221\n\t"
    "v_cvt_pk_bf16_f32 v145, v222, v223\n\t"
    "v_mfma_f32_16x16x32_bf16 v[120:123], a[36:39], v[164:167], v[120:123]\n\t"
    "s_add_u32 m0, s27, 114688\n\t"
    "v_cvt_pk_bf16_f32 v112, v112, v113\n\t"
    "v_cvt_pk_bf16_f32 v113, v114, v115\n\t"
    "v_mfma_f32_16x16x32_bf16 v[120:123], a[32:35], v[156:159], v[120:123]\n\t"
    "s_mov_b64 s[56:57], s[70:71]\n\t"
    "buffer_load_dwordx4 v40, s[28:31], s26 offen lds\n\t"
    "v_mfma_f32_16x16x32_bf16 v[120:123], a[28:31], v[152:155], v[120:123]\n\t"
    "ds_write_b64 v5, v[112:113] offset:12288\n\t"
    "v_mfma_f32_16x16x32_bf16 v[120:123], a[24:27], v[148:151], v[120:123]\n\t"
    "s_add_u32 m0, s27, 115712\n\t"
    "v_mul_f32 v248, v133, v248\n\t"
    "v_mul_f32 v249, v133, v249\n\t"
    "v_mfma_f32_16x16x32_bf16 v[212:215], a[48:51], v[148:151], 0\n\t"
    "ds_read_b128 v[228:231], v1 offset:4096\n\t"
    "v_mul_f32 v250, v133, v250\n\t"
    "v_mfma_f32_16x16x32_bf16 v[212:215], a[52:55], v[152:155], v[212:215]\n\t"
    "v_mul_f32 v251, v133, v251\n\t"
    "v_cvt_pk_bf16_f32 v146, v120, v121\n\t"
    "v_mfma_f32_16x16x32_bf16 v[212:215], a[56:59], v[156:159], v[212:215]\n\t"
    "buffer_load_dwordx4 v41, s[28:31], s26 offen lds\n\t"
    "v_mfma_f32_16x16x32_bf16 v[212:215], a[60:63], v[164:167], v[212:215]\n\t"
    "v_cvt_pk_bf16_f32 v147, v122, v123\n\t"
    "v_cvt_pk_bf16_f32 v248, v248, v249\n\t"
    "v_mfma_f32_16x16x32_bf16 v[212:215], a[64:67], v[168:171], v[212:215]\n\t"
    "s_add_u32 m0, s27, 116736\n\t"
    "ds_read_b128 v[252:255], v1 offset:12288\n\t"
    "v_cvt_pk_bf16_f32 v249, v250, v251\n\t"
    "v_mfma_f32_16x16x32_bf16 v[212:215], a[68:71], v[224:227], v[212:215]\n\t"
    "ds_write_b64 v5, v[248:249] offset:6144\n\t"
    "v_mfma_f32_16x16x32_bf16 v[124:127], a[92:95], v[224:227], 0\n\t"
    "ds_read_b128 v[248:251], v0 offset:12288\n\t"
    "v_mul_f32 v232, v136, v232\n\t"
    "v_mfma_f32_16x16x32_bf16 v[124:127], a[88:91], v[168:171], v[124:127]\n\t"
    "buffer_load_dwordx4 v42, s[28:31], s26 offen lds\n\t"
    "v_mfma_f32_16x16x32_bf16 v[124:127], a[84:87], v[164:167], v[124:127]\n\t"
    "v_mul_f32 v233, v136, v233\n\t"
    "v_cvt_pk_bf16_f32 v160, v212, v213\n\t"
    "v_mfma_f32_16x16x32_bf16 v[124:127], a[80:83], v[156:159], v[124:127]\n\t"
    "s_mul_i32 s26, s73, 3072\n\t"
    "v_cvt_pk_bf16_f32 v161, v214, v215\n\t"
    "v_mul_f32 v234, v136, v234\n\t"
    "v_mfma_f32_16x16x32_bf16 v[124:127], a[76:79], v[152:155], v[124:127]\n\t"
    "s_add_u32 s26, s26, 36864\n\t"
    "v_mul_f32 v235, v136, v235\n\t"
    "v_cvt_pk_bf16_f32 v232, v232, v233\n\t"
    "v_mfma_f32_16x16x32_bf16 v[124:127], a[72:75], v[148:151], v[124:127]\n\t"
    "s_add_u32 m0, s27, 126976\n\t"
    "v_cvt_pk_bf16_f32 v233, v234, v235\n\t"
    "v_mul_f32 v172, v136, v172\n\t"
    "v_mfma_f32_16x16x32_bf16 v[220:223], a[96:99], v[148:151], 0\n\t"
    "buffer_load_dwordx4 v40, s[28:31], s26 offen lds\n\t"
    "v_mfma_f32_16x16x32_bf16 v[220:223], a[100:103], v[152:155], v[220:223]\n\t"
    "v_mul_f32 v173, v136, v173\n\t"
    "v_mul_f32 v174, v136, v174\n\t"
    "v_mfma_f32_16x16x32_bf16 v[220:223], a[104:107], v[156:159], v[220:223]\n\t"
    "s_add_u32 m0, s27, 128000\n\t"
    "v_cvt_pk_bf16_f32 v112, v124, v125\n\t"
    "v_cvt_pk_bf16_f32 v113, v126, v127\n\t"
    "v_mfma_f32_16x16x32_bf16 v[220:223], a[108:111], v[164:167], v[220:223]\n\t"
    "v_mul_f32 v175, v136, v175\n\t"
    "v_cvt_pk_bf16_f32 v234, v172, v173\n\t"
    "v_mfma_f32_16x16x32_bf16 v[220:223], a[112:115], v[168:171], v[220:223]\n\t"
    "v_cvt_pk_bf16_f32 v235, v174, v175\n\t"
    "v_mul_f32 v236, v133, v236\n\t"
    "v_mfma_f32_16x16x32_bf16 v[220:223], a[116:119], v[224:227], v[220:223]\n\t"
    "buffer_load_dwordx4 v41, s[28:31], s26 offen lds\n\t"
    "v_mfma_f32_16x16x32_bf16 v[120:123], a[140:143], v[224:227], 0\n\t"
    "ds_read_b128 v[224:227], v0 offset:4096\n\t"
    "ds_read_b128 v[172:175], v1 offset:20480\n\t"
    "v_mfma_f32_16x16x32_bf16 v[120:123], a[136:139], v[168:171], v[120:123]\n\t"
    "s_add_u32 m0, s27, 129024\n\t"
    "ds_write_b128 v4, v[232:235] offset:0\n\t"
    "v_mfma_f32_16x16x32_bf16 v[120:123], a[132:135], v[164:167], v[120:123]\n\t"
    "v_cvt_pk_bf16_f32 v114, v220, v221\n\t"
    "v_cvt_pk_bf16_f32 v115, v222, v223\n\t"
    "v_mfma_f32_16x16x32_bf16 v[120:123], a[128:131], v[156:159], v[120:123]\n\t"
    "ds_read_b128 v[156:159], v0 offset:20480\n\t"
    "v_mul_f32 v237, v133, v237\n\t"
    "v_mfma_f32_16x16x32_bf16 v[120:123], a[124:127], v[152:155], v[120:123]\n\t"
    "buffer_load_dwordx4 v42, s[28:31], s26 offen lds\n\t"
    "v_mfma_f32_16x16x32_bf16 v[120:123], a[120:123], v[148:151], v[120:123]\n\t"
    "s_waitcnt lgkmcnt(14)\n\t"
    "v_mul_f32 v238, v133, v238\n\t"
    "v_mul_f32 v239, v133, v239\n\t"
    "v_mfma_f32_16x16x32_bf16 v[212:215], a[0:3], v[176:179], 0\n\t"
    "s_waitcnt lgkmcnt(13)\n\t"
    "s_mul_i32 s26, s73, 3072\n\t"
    "v_cvt_pk_bf16_f32 v236, v236, v237\n\t"
    "v_cvt_pk_bf16_f32 v237, v238, v239\n\t"
    "v_mfma_f32_16x16x32_bf16 v[212:215], a[4:7], v[180:183], v[212:215]\n\t"
    "s_waitcnt lgkmcnt(12)\n\t"
    "s_add_u32 s26, s26, 49152\n\t"
    "v_cvt_pk_bf16_f32 v128, v120, v121\n\t"
    "v_cvt_pk_bf16_f32 v129, v122, v123\n\t"
    "v_mfma_f32_16x16x32_bf16 v[212:215], a[8:11], v[184:187], v[212:215]\n\t"
    "s_waitcnt lgkmcnt(11)\n\t"
    "s_add_u32 m0, s27, 139264\n\t"
    "v_mul_f32 v200, v133, v200\n\t"
    "v_mul_f32 v201, v133, v201\n\t"
    "v_mfma_f32_16x16x32_bf16 v[212:215], a[12:15], v[188:191], v[212:215]\n\t"
    "s_waitcnt lgkmcnt(10)\n\t"
    "buffer_load_dwordx4 v40, s[28:31], s26 offen lds\n\t"
    "v_mfma_f32_16x16x32_bf16 v[212:215], a[16:19], v[192:195], v[212:215]\n\t"
    "s_waitcnt lgkmcnt(9)\n\t"
    "v_mul_f32 v202, v133, v202\n\t"
    "v_mul_f32 v203, v133, v203\n\t"
    "v_mfma_f32_16x16x32_bf16 v[212:215], a[20:23], v[196:199], v[212:215]\n\t"
    "s_add_u32 m0, s27, 140288\n\t"
    "v_cvt_pk_bf16_f32 v238, v200, v201\n\t"
    "v_cvt_pk_bf16_f32 v239, v202, v203\n\t"
    "v_mfma_f32_16x16x32_bf16 v[124:127], a[44:47], v[196:199], 0\n\t"
    "ds_write_b128 v4, v[236:239] offset:6144\n\t"
    "v_mfma_f32_16x16x32_bf16 v[124:127], a[40:43], v[192:195], v[124:127]\n\t"
    "v_mul_f32 v240, v140, v240\n\t"
    "v_mul_f32 v241, v140, v241\n\t"
    "v_mfma_f32_16x16x32_bf16 v[124:127], a[36:39], v[188:191], v[124:127]\n\t"
    "buffer_load_dwordx4 v41, s[28:31], s26 offen lds\n\t"
    "v_mfma_f32_16x16x32_bf16 v[124:127], a[32:35], v[184:187], v[124:127]\n\t"
    "v_cvt_pk_bf16_f32 v148, v212, v213\n\t"
    "v_cvt_pk_bf16_f32 v149, v214, v215\n\t"
    "v_mfma_f32_16x16x32_bf16 v[124:127], a[28:31], v[180:183], v[124:127]\n\t"
    "s_add_u32 m0, s27, 141312\n\t"
    "v_mul_f32 v242, v140, v242\n\t"
    "v_mul_f32 v243, v140, v243\n\t"
    "v_mfma_f32_16x16x32_bf16 v[124:127], a[24:27], v[176:179], v[124:127]\n\t"
    "v_cvt_pk_bf16_f32 v240, v240, v241\n\t"
    "v_cvt_pk_bf16_f32 v241, v242, v243\n\t"
    "v_mfma_f32_16x16x32_bf16 v[220:223], a[48:51], v[176:179], 0\n\t"
    "v_mul_f32 v204, v140, v204\n\t"
    "v_mul_f32 v205, v140, v205\n\t"
    "v_mfma_f32_16x16x32_bf16 v[220:223], a[52:55], v[180:183], v[220:223]\n\t"
    "buffer_load_dwordx4 v42, s[28:31], s26 offen lds\n\t"
    "v_mfma_f32_16x16x32_bf16 v[220:223], a[56:59], v[184:187], v[220:223]\n\t"
    "v_cvt_pk_bf16_f32 v150, v124, v125\n\t"
    "v_cvt_pk_bf16_f32 v151, v126, v127\n\t"
    "v_mfma_f32_16x16x32_bf16 v[220:223], a[60:63], v[188:191], v[220:223]\n\t"
    "s_mul_i32 s26, s73, 3072\n\t"
    "v_mul_f32 v206, v140, v206\n\t"
    "v_mul_f32 v207, v140, v207\n\t"
    "v_mfma_f32_16x16x32_bf16 v[220:223], a[64:67], v[192:195], v[220:223]\n\t"
    "s_add_u32 s26, s26, 61440\n\t"
    "v_cvt_pk_bf16_f32 v242, v204, v205\n\t"
    "v_cvt_pk_bf16_f32 v243, v206, v207\n\t"
    "v_mfma_f32_16x16x32_bf16 v[220:223], a[68:71], v[196:199], v[220:223]\n\t"
    "s_add_u32 m0, s27, 151552\n\t"
    "ds_write_b128 v4, v[240:243] offset:12288\n\t"
    "v_mfma_f32_16x16x32_bf16 v[120:123], a[92:95], v[196:199], 0\n\t"
    "buffer_load_dwordx4 v40, s[28:31], s26 offen lds\n\t"
    "v_mfma_f32_16x16x32_bf16 v[120:123], a[88:91], v[192:195], v[120:123]\n\t"
    "v_mul_f32 v244, v141, v244\n\t"
    "v_mul_f32 v245, v141, v245\n\t"
    "v_mfma_f32_16x16x32_bf16 v[120:123], a[84:87], v[188:191], v[120:123]\n\t"
    "s_add_u32 m0, s27, 152576\n\t"
    "v_mul_f32 v246, v141, v246\n\t"
    "v_mul_f32 v247, v141, v247\n\t"
    "v_mfma_f32_16x16x32_bf16 v[120:123], a[80:83], v[184:187], v[120:123]\n\t"
    "v_cvt_pk_bf16_f32 v244, v244, v245\n\t"
    "v_cvt_pk_bf16_f32 v245, v246, v247\n\t"
    "v_mfma_f32_16x16x32_bf16 v[120:123], a[76:79], v[180:183], v[120:123]\n\t"
    "v_mul_f32 v208, v141, v208\n\t"
    "v_mul_f32 v209, v141, v209\n\t"
    "v_mfma_f32_16x16x32_bf16 v[120:123], a[72:75], v[176:179], v[120:123]\n\t"
    "buffer_load_dwordx4 v41, s[28:31], s26 offen lds\n\t"
    "v_mfma_f32_16x16x32_bf16 v[212:215], a[96:99], v[176:179], 0\n\t"
    "v_mul_f32 v210, v141, v210\n\t"
    "v_mul_f32 v211, v141, v211\n\t"
    "v_mfma_f32_16x16x32_bf16 v[212:215], a[100:103], v[180:183], v[212:215]\n\t"
    "s_add_u32 m0, s27, 153600\n\t"
    "v_cvt_pk_bf16_f32 v246, v208, v209\n\t"
    "v_cvt_pk_bf16_f32 v247, v210, v211\n\t"
    "v_mfma_f32_16x16x32_bf16 v[212:215], a[104:107], v[184:187], v[212:215]\n\t"
    "ds_write_b128 v4, v[244:247] offset:18432\n\t"
    "v_mfma_f32_16x16x32_bf16 v[212:215], a[108:111], v[188:191], v[212:215]\n\t"
    "ds_read_b128 v[200:203], v0 offset:6144\n\t"
    "ds_read_b128 v[204:207], v1 offset:6144\n\t"
    "v_mfma_f32_16x16x32_bf16 v[212:215], a[112:115], v[192:195], v[212:215]\n\t"
    "buffer_load_dwordx4 v42, s[28:31], s26 offen lds\n\t"
    "v_mfma_f32_16x16x32_bf16 v[212:215], a[116:119], v[196:199], v[212:215]\n\t"
    "v_mul_f32 v116, v141, v116\n\t"
    "v_mul_f32 v117, v141, v117\n\t"
    "v_mfma_f32_16x16x32_bf16 v[124:127], a[140:143], v[196:199], 0\n\t"
    "ds_read_b128 v[208:211], v0 offset:14336\n\t"
    "v_mul_f32 v118, v141, v118\n\t"
    "v_mfma_f32_16x16x32_bf16 v[124:127], a[136:139], v[192:195], v[124:127]\n\t"
    "v_mul_f32 v119, v141, v119\n\t"
    "v_cvt_pk_bf16_f32 v116, v116, v117\n\t"
    "v_mfma_f32_16x16x32_bf16 v[124:127], a[132:135], v[188:191], v[124:127]\n\t"
    "v_cvt_pk_bf16_f32 v117, v118, v119\n\t"
    "v_cvt_pk_bf16_f32 v118, v212, v213\n\t"
    "v_mfma_f32_16x16x32_bf16 v[124:127], a[128:131], v[184:187], v[124:127]\n\t"
    "v_cvt_pk_bf16_f32 v119, v214, v215\n\t"
    "v_mul_f32 v216, v136, v216\n\t"
    "v_mfma_f32_16x16x32_bf16 v[124:127], a[124:127], v[180:183], v[124:127]\n\t"
    "ds_read_b128 v[212:215], v1 offset:14336\n\t"
    "v_mul_f32 v217, v136, v217\n\t"
    "v_mfma_f32_16x16x32_bf16 v[124:127], a[120:123], v[176:179], v[124:127]\n\t"
    "s_waitcnt lgkmcnt(10)\n\t"
    "v_mul_f32 v218, v136, v218\n\t"
    "v_mul_f32 v219, v136, v219\n\t"
    "v_mfma_f32_16x16x32_bf16 v[232:235], a[0:3], v[224:227], 0\n\t"
    "v_cvt_pk_bf16_f32 v216, v216, v217\n\t"
    "v_cvt_pk_bf16_f32 v217, v218, v219\n\t"
    "v_mfma_f32_16x16x32_bf16 v[232:235], a[4:7], v[228:231], v[232:235]\n\t"
    "ds_write_b64 v5, v[216:217] offset:0\n\t"
    "v_mfma_f32_16x16x32_bf16 v[232:235], a[8:11], v[248:251], v[232:235]\n\t"
    "ds_read_b128 v[216:219], v0 offset:22528\n\t"
    "v_cvt_pk_bf16_f32 v164, v220, v221\n\t"
    "v_mfma_f32_16x16x32_bf16 v[232:235], a[12:15], v[252:255], v[232:235]\n\t"
    "s_waitcnt lgkmcnt(9)\n\t"
    "v_cvt_pk_bf16_f32 v165, v222, v223\n\t"
    "v_cvt_pk_bf16_f32 v132, v124, v125\n\t"
    "v_mfma_f32_16x16x32_bf16 v[232:235], a[16:19], v[156:159], v[232:235]\n\t"
    "ds_read_b128 v[220:223], v1 offset:22528\n\t"
    "v_cvt_pk_bf16_f32 v133, v126, v127\n\t"
    "v_mfma_f32_16x16x32_bf16 v[232:235], a[20:23], v[172:175], v[232:235]\n\t"
    "ds_write_b64 v5, v[116:117] offset:18432\n\t"
    "v_mfma_f32_16x16x32_bf16 v[236:239], a[44:47], v[172:175], 0\n\t"
    "v_cvt_pk_bf16_f32 v116, v120, v121\n\t"
    "v_cvt_pk_bf16_f32 v117, v122, v123\n\t"
    "v_mfma_f32_16x16x32_bf16 v[236:239], a[40:43], v[156:159], v[236:239]\n\t"
    "ds_read_b64 v[162:163], v2 offset:0\n\t"
    "ds_read_b64 v[166:167], v2 offset:2048\n\t"
    "v_mfma_f32_16x16x32_bf16 v[236:239], a[36:39], v[252:255], v[236:239]\n\t"
    "v_cvt_pk_bf16_f32 v152, v232, v233\n\t"
    "v_cvt_pk_bf16_f32 v153, v234, v235\n\t"
    "v_mfma_f32_16x16x32_bf16 v[236:239], a[32:35], v[248:251], v[236:239]\n\t"
    "ds_read_b64 v[170:171], v2 offset:4096\n\t"
    "v_mfma_f32_16x16x32_bf16 v[236:239], a[28:31], v[228:231], v[236:239]\n\t"
    "v_mfma_f32_16x16x32_bf16 v[236:239], a[24:27], v[224:227], v[236:239]\n\t"
    "v_mfma_f32_16x16x32_bf16 v[240:243], a[48:51], v[224:227], 0\n\t"
    "v_mfma_f32_16x16x32_bf16 v[240:243], a[52:55], v[228:231], v[240:243]\n\t"
    "v_mfma_f32_16x16x32_bf16 v[240:243], a[56:59], v[248:251], v[240:243]\n\t"
    "v_mfma_f32_16x16x32_bf16 v[240:243], a[60:63], v[252:255], v[240:243]\n\t"
    "v_mfma_f32_16x16x32_bf16 v[240:243], a[64:67], v[156:159], v[240:243]\n\t"
    "v_mfma_f32_16x16x32_bf16 v[240:243], a[68:71], v[172:175], v[240:243]\n\t"
    "v_mfma_f32_16x16x32_bf16 v[244:247], a[92:95], v[172:175], 0\n\t"
    "v_mfma_f32_16x16x32_bf16 v[244:247], a[88:91], v[156:159], v[244:247]\n\t"
    "s_waitcnt vmcnt(0)\n\t"
    "s_barrier\n\t"
    "v_cvt_pk_bf16_f32 v154, v236, v237\n\t"
    "v_cvt_pk_bf16_f32 v155, v238, v239\n\t"
    "v_mfma_f32_16x16x32_bf16 v[244:247], a[84:87], v[252:255], v[244:247]\n\t"
    "ds_read_b64_tr_b16 a[216:217], v44 offset:0\n\t"
    "ds_read_b64_tr_b16 a[218:219], v44 offset:6144\n\t"
    "v_mfma_f32_16x16x32_bf16 v[244:247], a[80:83], v[248:251], v[244:247]\n\t"
    "v_cvt_pk_bf16_f32 v168, v240, v241\n\t"
    "v_cvt_pk_bf16_f32 v169, v242, v243\n\t"
    "v_mfma_f32_16x16x32_bf16 v[244:247], a[76:79], v[228:231], v[244:247]\n\t"
    "ds_read_b64_tr_b16 a[220:221], v44 offset:18432\n\t"
    "ds_read_b64_tr_b16 a[222:223], v44 offset:24576\n\t"
    "v_mfma_f32_16x16x32_bf16 v[244:247], a[72:75], v[224:227], v[244:247]\n\t"
    "ds_read_b64_tr_b16 a[224:225], v46 offset:0\n\t"
    "ds_read_b64_tr_b16 a[226:227], v46 offset:6144\n\t"
    "v_mfma_f32_16x16x32_bf16 v[232:235], a[96:99], v[224:227], 0\n\t"
    "ds_read_b64_tr_b16 a[228:229], v46 offset:18432\n\t"
    "ds_read_b64_tr_b16 a[230:231], v46 offset:24576\n\t"
    "v_mfma_f32_16x16x32_bf16 v[232:235], a[100:103], v[228:231], v[232:235]\n\t"
    "ds_read_b64_tr_b16 a[232:233], v44 offset:12288\n\t"
    "ds_read_b64_tr_b16 a[234:235], v44 offset:30720\n\t"
    "v_mfma_f32_16x16x32_bf16 v[232:235], a[104:107], v[248:251], v[232:235]\n\t"
    "s_add_u32 m0, s61, 0\n\t"
    "v_cvt_pk_bf16_f32 v120, v244, v245\n\t"
    "v_cvt_pk_bf16_f32 v121, v246, v247\n\t"
    "v_mfma_f32_16x16x32_bf16 v[232:235], a[108:111], v[252:255], v[232:235]\n\t"
    "buffer_load_dwordx4 v9, s[36:39], s48 offen nt lds\n\t"
    "v_mfma_f32_16x16x32_bf16 v[232:235], a[112:115], v[156:159], v[232:235]\n\t"
    "ds_read_b64_tr_b16 a[236:237], v46 offset:12288\n\t"
    "ds_read_b64_tr_b16 a[238:239], v46 offset:30720\n\t"
    "v_mfma_f32_16x16x32_bf16 v[232:235], a[116:119], v[172:175], v[232:235]\n\t"
    "ds_read_b64_tr_b16 a[240:241], v44 offset:8\n\t"
    "ds_read_b64_tr_b16 a[242:243], v44 offset:6152\n\t"
    "v_mfma_f32_16x16x32_bf16 v[236:239], a[140:143], v[172:175], 0\n\t"
    "ds_read_b64_tr_b16 a[244:245], v44 offset:18440\n\t"
    "ds_read_b64_tr_b16 a[246:247], v44 offset:24584\n\t"
    "v_mfma_f32_16x16x32_bf16 v[236:239], a[136:139], v[156:159], v[236:239]\n\t"
    "s_add_u32 m0, s61, 8064\n\t"
    "ds_read_b64_tr_b16 a[248:249], v46 offset:8\n\t"
    "v_cvt_pk_bf16_f32 v122, v232, v233\n\t"
    "v_mfma_f32_16x16x32_bf16 v[236:239], a[132:135], v[252:255], v[236:239]\n\t"
    "buffer_load_dwordx4 v9, s[36:39], s48 offen offset:128 nt lds\n\t"
    "v_mfma_f32_16x16x32_bf16 v[236:239], a[128:131], v[248:251], v[236:239]\n\t"
    "ds_read_b64_tr_b16 a[250:251], v46 offset:6152\n\t"
    "v_cvt_pk_bf16_f32 v123, v234, v235\n\t"
    "v_mfma_f32_16x16x32_bf16 v[236:239], a[124:127], v[228:231], v[236:239]\n\t"
    "ds_read_b64_tr_b16 a[252:253], v46 offset:18440\n\t"
    "ds_read_b64_tr_b16 a[254:255], v46 offset:24584\n\t"
    "v_mfma_f32_16x16x32_bf16 v[236:239], a[120:123], v[224:227], v[236:239]\n\t"
    "s_waitcnt lgkmcnt(13)\n\t"
    "ds_read_b64_tr_b16 v[16:17], v44 offset:12296\n\t"
    "ds_read_b64_tr_b16 v[18:19], v44 offset:30728\n\t"
    "v_mfma_f32_16x16x32_bf16 v[240:243], a[0:3], v[200:203], 0\n\t"
    "s_add_u32 m0, s61, 16128\n\t"
    "ds_read_b64_tr_b16 v[20:21], v46 offset:12296\n\t"
    "ds_read_b64_tr_b16 v[22:23], v46 offset:30728\n\t"
    "v_mfma_f32_16x16x32_bf16 v[240:243], a[4:7], v[204:207], v[240:243]\n\t"
    "buffer_load_dwordx4 v9, s[36:39], s48 offen offset:256 nt lds\n\t"
    "v_mfma_f32_16x16x32_bf16 v[240:243], a[8:11], v[208:211], v[240:243]\n\t"
    "v_cvt_pk_bf16_f32 v136, v236, v237\n\t"
    "v_cvt_pk_bf16_f32 v137, v238, v239\n\t"
    "v_mfma_f32_16x16x32_bf16 v[240:243], a[12:15], v[212:215], v[240:243]\n\t"
    "s_add_u32 s59, s59, 16\n\t"
    "ds_read_b64_tr_b16 v[24:25], v45 offset:0\n\t"
    "ds_read_b64_tr_b16 v[26:27], v45 offset:6144\n\t"
    "v_mfma_f32_16x16x32_bf16 v[240:243], a[16:19], v[216:219], v[240:243]\n\t"
    "s_cmp_ge_u32 s59, 48\n\t"
    "s_cselect_b32 s63, 48, 0\n\t"
    "s_addc_u32 s58, s58, 5\n\t"
    "ds_read_b64_tr_b16 v[28:29], v45 offset:18432\n\t"
    "ds_read_b64_tr_b16 v[30:31], v45 offset:24576\n\t"
    "v_mfma_f32_16x16x32_bf16 v[240:243], a[20:23], v[220:223], v[240:243]\n\t"
    "s_add_u32 m0, s61, 4096\n\t"
    "ds_read_b64_tr_b16 v[32:33], v47 offset:0\n\t"
    "ds_read_b64_tr_b16 v[34:35], v47 offset:6144\n\t"
    "v_mfma_f32_16x16x32_bf16 v[244:247], a[44:47], v[220:223], 0\n\t"
    "s_sub_u32 s59, s59, s63\n\t"
    "buffer_load_dwordx4 v9, s[36:39], s49 offen nt lds\n\t"
    "v_mfma_f32_16x16x32_bf16 v[244:247], a[40:43], v[216:219], v[244:247]\n\t"
    "s_lshl_b32 s64, s59, 3\n\t"
    "v_cvt_pk_bf16_f32 v156, v240, v241\n\t"
    "v_cvt_pk_bf16_f32 v157, v242, v243\n\t"
    "v_mfma_f32_16x16x32_bf16 v[244:247], a[36:39], v[212:215], v[244:247]\n\t"
    "s_lshl_b32 s63, s58, 3\n\t"
    "ds_read_b64_tr_b16 v[36:37], v47 offset:18432\n\t"
    "ds_read_b64_tr_b16 v[38:39], v47 offset:24576\n\t"
    "v_mfma_f32_16x16x32_bf16 v[244:247], a[32:35], v[208:211], v[244:247]\n\t"
    "s_add_u32 s64, s64, s66\n\t"
    "ds_read_b64_tr_b16 v[40:41], v45 offset:12288\n\t"
    "ds_read_b64_tr_b16 v[42:43], v45 offset:30720\n\t"
    "v_mfma_f32_16x16x32_bf16 v[244:247], a[28:31], v[204:207], v[244:247]\n\t"
    "s_add_u32 m0, s61, 12160\n\t"
    "ds_read_b64_tr_b16 v[44:45], v47 offset:12288\n\t"
    "ds_read_b64_tr_b16 v[46:47], v47 offset:30720\n\t"
    "v_mfma_f32_16x16x32_bf16 v[244:247], a[24:27], v[200:203], v[244:247]\n\t"
    "s_add_u32 s63, s63, s65\n\t"
    "buffer_load_dwordx4 v9, s[36:39], s49 offen offset:128 nt lds\n\t"
    "v_mfma_f32_16x16x32_bf16 v[232:235], a[48:51], v[200:203], 0\n\t"
    "s_cmp_eq_u32 s64, 384\n\t"
    "s_cselect_b32 s64, 0, s64\n\t"
    "ds_read_b64 v[174:175], v2 offset:6144\n\t"
    "v_mfma_f32_16x16x32_bf16 v[232:235], a[52:55], v[204:207], v[232:235]\n\t"
    "s_add_u32 s72, s72, 256\n\t"
    "v_mfma_f32_16x16x32_bf16 v[232:235], a[56:59], v[208:211], v[232:235]\n\t"
    "s_mul_i32 s64, s64, 384\n\t"
    "v_cvt_pk_bf16_f32 v158, v244, v245\n\t"
    "v_cvt_pk_bf16_f32 v159, v246, v247\n\t"
    "v_mfma_f32_16x16x32_bf16 v[232:235], a[60:63], v[212:215], v[232:235]\n\t"
    "s_add_u32 m0, s61, 20224\n\t"
    "v_mfma_f32_16x16x32_bf16 v[232:235], a[64:67], v[216:219], v[232:235]\n\t"
    "s_mul_i32 s68, s63, 147456\n\t"
    "buffer_load_dwordx4 v9, s[36:39], s49 offen offset:256 nt lds\n\t"
    "v_mfma_f32_16x16x32_bf16 v[232:235], a[68:71], v[220:223], v[232:235]\n\t"
    "s_add_u32 s68, s68, s64\n\t"
    "v_mfma_f32_16x16x32_bf16 v[236:239], a[92:95], v[220:223], 0\n\t"
    "s_lshl_b32 s70, s72, 13\n\t"
    "v_mfma_f32_16x16x32_bf16 v[236:239], a[88:91], v[216:219], v[236:239]\n\t"
    "s_cmp_ge_u32 s63, 380\n\t"
    "s_cselect_b32 s63, 56623104, 0\n\t"
    "v_mfma_f32_16x16x32_bf16 v[236:239], a[84:87], v[212:215], v[236:239]\n\t"
    "s_add_u32 m0, s61, 24576\n\t"
    "v_mfma_f32_16x16x32_bf16 v[236:239], a[80:83], v[208:211], v[236:239]\n\t"
    "s_add_u32 s69, s68, 589824\n\t"
    "buffer_load_dwordx4 v10, s[40:43], s56 offen nt lds\n\t"
    "v_mfma_f32_16x16x32_bf16 v[236:239], a[76:79], v[204:207], v[236:239]\n\t"
    "s_add_u32 s71, s70, 4096\n\t"
    "v_cvt_pk_bf16_f32 v172, v232, v233\n\t"
    "v_cvt_pk_bf16_f32 v173, v234, v235\n\t"
    "v_mfma_f32_16x16x32_bf16 v[236:239], a[72:75], v[200:203], v[236:239]\n\t"
    "s_sub_u32 s69, s69, s63\n\t"
    "v_mfma_f32_16x16x32_bf16 v[240:243], a[96:99], v[200:203], 0\n\t"
    "s_waitcnt lgkmcnt(1)\n\t"
    "s_barrier\n\t"
    "v_mfma_f32_16x16x32_bf16 v[240:243], a[100:103], v[204:207], v[240:243]\n\t"
    "s_add_u32 m0, s61, 28672\n\t"
    "v_mfma_f32_16x16x32_bf16 v[240:243], a[104:107], v[208:211], v[240:243]\n\t"
    "buffer_load_dwordx4 v10, s[40:43], s57 offen nt lds\n\t"
    "v_mfma_f32_16x16x32_bf16 v[240:243], a[108:111], v[212:215], v[240:243]\n\t"
    "s_cmp_lt_u32 s60, 7\n\t"
    "s_cselect_b32 s38, 56623104, 0\n\t"
    "s_cselect_b32 s42, s42, s18\n\t"
    "v_cvt_pk_bf16_f32 v124, v236, v237\n\t"
    "v_cvt_pk_bf16_f32 v125, v238, v239\n\t"
    "v_mfma_f32_16x16x32_bf16 v[240:243], a[112:115], v[216:219], v[240:243]\n\t"
    "s_cmp_lt_u32 s60, 7\n\t"
    "s_cselect_b64 s[40:41], s[40:41], s[24:25]\n\t"
    "s_cselect_b64 s[70:71], s[70:71], s[16:17]\n\t"
    "v_mfma_f32_16x16x32_bf16 v[240:243], a[116:119], v[220:223], v[240:243]\n\t"
    "v_mfma_f32_16x16x32_bf16 v[244:247], a[140:143], v[220:223], 0\n\t"
    "v_mfma_f32_16x16x32_bf16 v[244:247], a[136:139], v[216:219], v[244:247]\n\t"
    "v_mfma_f32_16x16x32_bf16 v[244:247], a[132:135], v[212:215], v[244:247]\n\t"
    "v_mfma_f32_16x16x32_bf16 v[244:247], a[128:131], v[208:211], v[244:247]\n\t"
    "v_mfma_f32_16x16x32_bf16 v[244:247], a[124:127], v[204:207], v[244:247]\n\t"
    "v_mfma_f32_16x16x32_bf16 v[244:247], a[120:123], v[200:203], v[244:247]\n\t"
    "s_waitcnt lgkmcnt(0)\n\t"
    "s_branch ENTRY_%=\n\t"
    "LOOP_%=:\n\t"
    "s_mov_b64 s[54:55], s[52:53]\n\t"
    "s_waitcnt lgkmcnt(6)\n\t"
    "v_mfma_f32_16x16x32_bf16 v[220:223], a[0:3], v[148:151], 0\n\t"
    "s_waitcnt lgkmcnt(4)\n\t"
    "s_mov_b64 s[52:53], s[50:51]\n\t"
    "ds_read_b128 v[176:179], v0 offset:2048\n\t"
    "ds_read_b128 v[180:183], v1 offset:2048\n\t"
    "v_mfma_f32_16x16x32_bf16 v[220:223], a[4:7], v[152:155], v[220:223]\n\t"
    "s_waitcnt lgkmcnt(3)\n\t"
    "s_mov_b64 s[50:51], s[48:49]\n\t"
    "ds_read_b128 v[184:187], v0 offset:10240\n\t"
    "ds_read_b128 v[188:191], v1 offset:10240\n\t"
    "v_mfma_f32_16x16x32_bf16 v[220:223], a[8:11], v[156:159], v[220:223]\n\t"
    "s_xor_b32 s61, s61, 0x8000\n\t"
    "ds_read_b128 v[192:195], v0 offset:18432\n\t"
    "ds_read_b128 v[196:199], v1 offset:18432\n\t"
    "v_mfma_f32_16x16x32_bf16 v[220:223], a[12:15], v[164:167], v[220:223]\n\t"
    "s_add_u32 m0, s61, 0\n\t"
    "v_mul_f32 v112, v140, v112\n\t"
    "v_mul_f32 v113, v140, v113\n\t"
    "v_mfma_f32_16x16x32_bf16 v[220:223], a[16:19], v[168:171], v[220:223]\n\t"
    "s_mov_b64 s[48:49], s[68:69]\n\t"
    "buffer_load_dwordx4 v9, s[36:39], s48 offen nt lds\n\t"
    "v_mfma_f32_16x16x32_bf16 v[220:223], a[20:23], v[224:227], v[220:223]\n\t"
    "s_mov_b64 s[56:57], s[70:71]\n\t"
    "v_mul_f32 v114, v140, v114\n\t"
    "v_mul_f32 v115, v140, v115\n\t"
    "v_mfma_f32_16x16x32_bf16 v[120:123], a[44:47], v[224:227], 0\n\t"
    "s_mov_b32 s46, 56623104\n\t"
    "v_cvt_pk_bf16_f32 v112, v112, v113\n\t"
    "v_cvt_pk_bf16_f32 v113, v114, v115\n\t"
    "v_mfma_f32_16x16x32_bf16 v[120:123], a[40:43], v[168:171], v[120:123]\n\t"
    "v_cvt_pk_bf16_f32 v144, v220, v221\n\t"
    "v_cvt_pk_bf16_f32 v145, v222, v223\n\t"
    "v_mfma_f32_16x16x32_bf16 v[120:123], a[36:39], v[164:167], v[120:123]\n\t"
    "ds_write_b64 v5, v[112:113] offset:12288\n\t"
    "v_mfma_f32_16x16x32_bf16 v[120:123], a[32:35], v[156:159], v[120:123]\n\t"
    "v_mul_f32 v248, v133, v248\n\t"
    "v_mul_f32 v249, v133, v249\n\t"
    "v_mfma_f32_16x16x32_bf16 v[120:123], a[28:31], v[152:155], v[120:123]\n\t"
    "ds_read_b128 v[228:231], v1 offset:4096\n\t"
    "v_mul_f32 v250, v133, v250\n\t"
    "v_mfma_f32_16x16x32_bf16 v[120:123], a[24:27], v[148:151], v[120:123]\n\t"
    "v_mul_f32 v251, v133, v251\n\t"
    "v_cvt_pk_bf16_f32 v248, v248, v249\n\t"
    "v_mfma_f32_16x16x32_bf16 v[212:215], a[48:51], v[148:151], 0\n\t"
    "ds_read_b128 v[252:255], v1 offset:12288\n\t"
    "v_cvt_pk_bf16_f32 v249, v250, v251\n\t"
    "v_mfma_f32_16x16x32_bf16 v[212:215], a[52:55], v[152:155], v[212:215]\n\t"
    "ds_write_b64 v5, v[248:249] offset:6144\n\t"
    "v_mfma_f32_16x16x32_bf16 v[212:215], a[56:59], v[156:159], v[212:215]\n\t"
    "v_cvt_pk_bf16_f32 v146, v120, v121\n\t"
    "v_cvt_pk_bf16_f32 v147, v122, v123\n\t"
    "v_mfma_f32_16x16x32_bf16 v[212:215], a[60:63], v[164:167], v[212:215]\n\t"
    "ds_read_b128 v[248:251], v0 offset:12288\n\t"
    "v_mul_f32 v232, v136, v232\n\t"
    "v_mfma_f32_16x16x32_bf16 v[212:215], a[64:67], v[168:171], v[212:215]\n\t"
    "v_mul_f32 v233, v136, v233\n\t"
    "v_mul_f32 v234, v136, v234\n\t"
    "v_mfma_f32_16x16x32_bf16 v[212:215], a[68:71], v[224:227], v[212:215]\n\t"
    "v_mul_f32 v235, v136, v235\n\t"
    "v_cvt_pk_bf16_f32 v232, v232, v233\n\t"
    "v_mfma_f32_16x16x32_bf16 v[124:127], a[92:95], v[224:227], 0\n\t"
    "v_cvt_pk_bf16_f32 v233, v234, v235\n\t"
    "v_mul_f32 v172, v136, v172\n\t"
    "v_mfma_f32_16x16x32_bf16 v[124:127], a[88:91], v[168:171], v[124:127]\n\t"
    "v_mul_f32 v173, v136, v173\n\t"
    "v_mul_f32 v174, v136, v174\n\t"
    "v_mfma_f32_16x16x32_bf16 v[124:127], a[84:87], v[164:167], v[124:127]\n\t"
    "s_add_u32 m0, s61, 8064\n\t"
    "v_cvt_pk_bf16_f32 v160, v212, v213\n\t"
    "v_cvt_pk_bf16_f32 v161, v214, v215\n\t"
    "v_mfma_f32_16x16x32_bf16 v[124:127], a[80:83], v[156:159], v[124:127]\n\t"
    "buffer_load_dwordx4 v9, s[36:39], s48 offen offset:128 nt lds\n\t"
    "v_mfma_f32_16x16x32_bf16 v[124:127], a[76:79], v[152:155], v[124:127]\n\t"
    "v_mul_f32 v175, v136, v175\n\t"
    "v_cvt_pk_bf16_f32 v234, v172, v173\n\t"
    "v_mfma_f32_16x16x32_bf16 v[124:127], a[72:75], v[148:151], v[124:127]\n\t"
    "v_cvt_pk_bf16_f32 v235, v174, v175\n\t"
    "v_mul_f32 v236, v133, v236\n\t"
    "v_mfma_f32_16x16x32_bf16 v[220:223], a[96:99], v[148:151], 0\n\t"
    "ds_read_b128 v[172:175], v1 offset:20480\n\t"
    "v_mul_f32 v237, v133, v237\n\t"
    "v_mfma_f32_16x16x32_bf16 v[220:223], a[100:103], v[152:155], v[220:223]\n\t"
    "ds_write_b128 v4, v[232:235] offset:0\n\t"
    "v_mfma_f32_16x16x32_bf16 v[220:223], a[104:107], v[156:159], v[220:223]\n\t"
    "v_cvt_pk_bf16_f32 v112, v124, v125\n\t"
    "v_cvt_pk_bf16_f32 v113, v126, v127\n\t"
    "v_mfma_f32_16x16x32_bf16 v[220:223], a[108:111], v[164:167], v[220:223]\n\t"
    "v_mul_f32 v238, v133, v238\n\t"
    "v_mul_f32 v239, v133, v239\n\t"
    "v_mfma_f32_16x16x32_bf16 v[220:223], a[112:115], v[168:171], v[220:223]\n\t"
    "v_cvt_pk_bf16_f32 v236, v236, v237\n\t"
    "v_cvt_pk_bf16_f32 v237, v238, v239\n\t"
    "v_mfma_f32_16x16x32_bf16 v[220:223], a[116:119], v[224:227], v[220:223]\n\t"
    "v_mul_f32 v200, v133, v200\n\t"
    "v_mul_f32 v201, v133, v201\n\t"
    "v_mfma_f32_16x16x32_bf16 v[120:123], a[140:143], v[224:227], 0\n\t"
    "ds_read_b128 v[224:227], v0 offset:4096\n\t"
    "v_mul_f32 v202, v133, v202\n\t"
    "v_mfma_f32_16x16x32_bf16 v[120:123], a[136:139], v[168:171], v[120:123]\n\t"
    "v_mul_f32 v203, v133, v203\n\t"
    "v_cvt_pk_bf16_f32 v238, v200, v201\n\t"
    "v_mfma_f32_16x16x32_bf16 v[120:123], a[132:135], v[164:167], v[120:123]\n\t"
    "v_cvt_pk_bf16_f32 v114, v220, v221\n\t"
    "v_cvt_pk_bf16_f32 v115, v222, v223\n\t"
    "v_mfma_f32_16x16x32_bf16 v[120:123], a[128:131], v[156:159], v[120:123]\n\t"
    "ds_read_b128 v[156:159], v0 offset:20480\n\t"
    "v_cvt_pk_bf16_f32 v239, v202, v203\n\t"
    "v_mfma_f32_16x16x32_bf16 v[120:123], a[124:127], v[152:155], v[120:123]\n\t"
    "ds_write_b128 v4, v[236:239] offset:6144\n\t"
    "v_mfma_f32_16x16x32_bf16 v[120:123], a[120:123], v[148:151], v[120:123]\n\t"
    "s_waitcnt lgkmcnt(15)\n\t"
    "v_mul_f32 v240, v140, v240\n\t"
    "v_mul_f32 v241, v140, v241\n\t"
    "v_mfma_f32_16x16x32_bf16 v[212:215], a[0:3], v[176:179], 0\n\t"
    "s_waitcnt lgkmcnt(14)\n\t"
    "v_mul_f32 v242, v140, v242\n\t"
    "v_mul_f32 v243, v140, v243\n\t"
    "v_mfma_f32_16x16x32_bf16 v[212:215], a[4:7], v[180:183], v[212:215]\n\t"
    "s_waitcnt lgkmcnt(13)\n\t"
    "v_cvt_pk_bf16_f32 v240, v240, v241\n\t"
    "v_cvt_pk_bf16_f32 v241, v242, v243\n\t"
    "v_mfma_f32_16x16x32_bf16 v[212:215], a[8:11], v[184:187], v[212:215]\n\t"
    "s_waitcnt lgkmcnt(12)\n\t"
    "s_add_u32 m0, s61, 16128\n\t"
    "v_cvt_pk_bf16_f32 v128, v120, v121\n\t"
    "v_cvt_pk_bf16_f32 v129, v122, v123\n\t"
    "v_mfma_f32_16x16x32_bf16 v[212:215], a[12:15], v[188:191], v[212:215]\n\t"
    "s_waitcnt lgkmcnt(11)\n\t"
    "buffer_load_dwordx4 v9, s[36:39], s48 offen offset:256 nt lds\n\t"
    "v_mfma_f32_16x16x32_bf16 v[212:215], a[16:19], v[192:195], v[212:215]\n\t"
    "s_waitcnt lgkmcnt(10)\n\t"
    "v_mul_f32 v204, v140, v204\n\t"
    "v_mul_f32 v205, v140, v205\n\t"
    "v_mfma_f32_16x16x32_bf16 v[212:215], a[20:23], v[196:199], v[212:215]\n\t"
    "v_mul_f32 v206, v140, v206\n\t"
    "v_mul_f32 v207, v140, v207\n\t"
    "v_mfma_f32_16x16x32_bf16 v[124:127], a[44:47], v[196:199], 0\n\t"
    "v_cvt_pk_bf16_f32 v242, v204, v205\n\t"
    "v_cvt_pk_bf16_f32 v243, v206, v207\n\t"
    "v_mfma_f32_16x16x32_bf16 v[124:127], a[40:43], v[192:195], v[124:127]\n\t"
    "ds_write_b128 v4, v[240:243] offset:12288\n\t"
    "v_mfma_f32_16x16x32_bf16 v[124:127], a[36:39], v[188:191], v[124:127]\n\t"
    "v_cvt_pk_bf16_f32 v148, v212, v213\n\t"
    "v_cvt_pk_bf16_f32 v149, v214, v215\n\t"
    "v_mfma_f32_16x16x32_bf16 v[124:127], a[32:35], v[184:187], v[124:127]\n\t"
    "v_mul_f32 v244, v141, v244\n\t"
    "v_mul_f32 v245, v141, v245\n\t"
    "v_mfma_f32_16x16x32_bf16 v[124:127], a[28:31], v[180:183], v[124:127]\n\t"
    "v_mul_f32 v246, v141, v246\n\t"
    "v_mul_f32 v247, v141, v247\n\t"
    "v_mfma_f32_16x16x32_bf16 v[124:127], a[24:27], v[176:179], v[124:127]\n\t"
    "v_cvt_pk_bf16_f32 v244, v244, v245\n\t"
    "v_cvt_pk_bf16_f32 v245, v246, v247\n\t"
    "v_mfma_f32_16x16x32_bf16 v[220:223], a[48:51], v[176:179], 0\n\t"
    "v_mul_f32 v208, v141, v208\n\t"
    "v_mul_f32 v209, v141, v209\n\t"
    "v_mfma_f32_16x16x32_bf16 v[220:223], a[52:55], v[180:183], v[220:223]\n\t"
    "v_mul_f32 v210, v141, v210\n\t"
    "v_mul_f32 v211, v141, v211\n\t"
    "v_mfma_f32_16x16x32_bf16 v[220:223], a[56:59], v[184:187], v[220:223]\n\t"
    "v_cvt_pk_bf16_f32 v150, v124, v125\n\t"
    "v_cvt_pk_bf16_f32 v151, v126, v127\n\t"
    "v_mfma_f32_16x16x32_bf16 v[220:223], a[60:63], v[188:191], v[220:223]\n\t"
    "v_cvt_pk_bf16_f32 v246, v208, v209\n\t"
    "v_cvt_pk_bf16_f32 v247, v210, v211\n\t"
    "v_mfma_f32_16x16x32_bf16 v[220:223], a[64:67], v[192:195], v[220:223]\n\t"
    "ds_write_b128 v4, v[244:247] offset:18432\n\t"
    "v_mfma_f32_16x16x32_bf16 v[220:223], a[68:71], v[196:199], v[220:223]\n\t"
    "ds_read_b128 v[200:203], v0 offset:6144\n\t"
    "ds_read_b128 v[204:207], v1 offset:6144\n\t"
    "v_mfma_f32_16x16x32_bf16 v[120:123], a[92:95], v[196:199], 0\n\t"
    "v_mul_f32 v116, v141, v116\n\t"
    "v_mul_f32 v117, v141, v117\n\t"
    "v_mfma_f32_16x16x32_bf16 v[120:123], a[88:91], v[192:195], v[120:123]\n\t"
    "ds_read_b128 v[208:211], v0 offset:14336\n\t"
    "v_mul_f32 v118, v141, v118\n\t"
    "v_mfma_f32_16x16x32_bf16 v[120:123], a[84:87], v[188:191], v[120:123]\n\t"
    "s_add_u32 m0, s61, 4096\n\t"
    "v_mul_f32 v119, v141, v119\n\t"
    "v_cvt_pk_bf16_f32 v116, v116, v117\n\t"
    "v_mfma_f32_16x16x32_bf16 v[120:123], a[80:83], v[184:187], v[120:123]\n\t"
    "buffer_load_dwordx4 v9, s[36:39], s49 offen nt lds\n\t"
    "v_mfma_f32_16x16x32_bf16 v[120:123], a[76:79], v[180:183], v[120:123]\n\t"
    "v_cvt_pk_bf16_f32 v117, v118, v119\n\t"
    "v_mul_f32 v216, v136, v216\n\t"
    "v_mfma_f32_16x16x32_bf16 v[120:123], a[72:75], v[176:179], v[120:123]\n\t"
    "v_mul_f32 v217, v136, v217\n\t"
    "v_mul_f32 v218, v136, v218\n\t"
    "v_mfma_f32_16x16x32_bf16 v[212:215], a[96:99], v[176:179], 0\n\t"
    "v_mul_f32 v219, v136, v219\n\t"
    "v_cvt_pk_bf16_f32 v216, v216, v217\n\t"
    "v_mfma_f32_16x16x32_bf16 v[212:215], a[100:103], v[180:183], v[212:215]\n\t"
    "v_cvt_pk_bf16_f32 v217, v218, v219\n\t"
    "v_cvt_pk_bf16_f32 v164, v220, v221\n\t"
    "v_mfma_f32_16x16x32_bf16 v[212:215], a[104:107], v[184:187], v[212:215]\n\t"
    "ds_write_b64 v5, v[216:217] offset:0\n\t"
    "v_mfma_f32_16x16x32_bf16 v[212:215], a[108:111], v[188:191], v[212:215]\n\t"
    "ds_read_b128 v[216:219], v0 offset:22528\n\t"
    "v_cvt_pk_bf16_f32 v165, v222, v223\n\t"
    "v_mfma_f32_16x16x32_bf16 v[212:215], a[112:115], v[192:195], v[212:215]\n\t"
    "ds_read_b128 v[220:223], v1 offset:22528\n\t"
    "ds_read_b64 v[162:163], v2 offset:0\n\t"
    "v_mfma_f32_16x16x32_bf16 v[212:215], a[116:119], v[196:199], v[212:215]\n\t"
    "ds_write_b64 v5, v[116:117] offset:18432\n\t"
    "v_mfma_f32_16x16x32_bf16 v[124:127], a[140:143], v[196:199], 0\n\t"
    "v_cvt_pk_bf16_f32 v116, v120, v121\n\t"
    "v_cvt_pk_bf16_f32 v117, v122, v123\n\t"
    "v_mfma_f32_16x16x32_bf16 v[124:127], a[136:139], v[192:195], v[124:127]\n\t"
    "ds_read_b64 v[166:167], v2 offset:2048\n\t"
    "ds_read_b64 v[170:171], v2 offset:4096\n\t"
    "v_mfma_f32_16x16x32_bf16 v[124:127], a[132:135], v[188:191], v[124:127]\n\t"
    "v_cvt_pk_bf16_f32 v118, v212, v213\n\t"
    "v_cvt_pk_bf16_f32 v119, v214, v215\n\t"
    "v_mfma_f32_16x16x32_bf16 v[124:127], a[128:131], v[184:187], v[124:127]\n\t"
    "ds_read_b128 v[212:215], v1 offset:14336\n\t"
    "v_mfma_f32_16x16x32_bf16 v[124:127], a[124:127], v[180:183], v[124:127]\n\t"
    "v_mfma_f32_16x16x32_bf16 v[124:127], a[120:123], v[176:179], v[124:127]\n\t"
    "s_waitcnt lgkmcnt(15)\n\t"
    "v_mfma_f32_16x16x32_bf16 v[232:235], a[0:3], v[224:227], 0\n\t"
    "s_waitcnt lgkmcnt(3)\n\t"
    "s_barrier\n\t"
    "v_mfma_f32_16x16x32_bf16 v[232:235], a[4:7], v[228:231], v[232:235]\n\t"
    "ds_read_b128 v[176:179], v8 offset:0\n\t"
    "v_mfma_f32_16x16x32_bf16 v[232:235], a[8:11], v[248:251], v[232:235]\n\t"
    "s_add_u32 m0, s61, 12160\n\t"
    "v_mfma_f32_16x16x32_bf16 v[232:235], a[12:15], v[252:255], v[232:235]\n\t"
    "buffer_load_dwordx4 v9, s[36:39], s49 offen offset:128 nt lds\n\t"
    "v_mfma_f32_16x16x32_bf16 v[232:235], a[16:19], v[156:159], v[232:235]\n\t"
    "v_cvt_pk_bf16_f32 v132, v124, v125\n\t"
    "v_cvt_pk_bf16_f32 v133, v126, v127\n\t"
    "v_mfma_f32_16x16x32_bf16 v[232:235], a[20:23], v[172:175], v[232:235]\n\t"
    "v_mfma_f32_16x16x32_bf16 v[236:239], a[44:47], v[172:175], 0\n\t"
    "v_mfma_f32_16x16x32_bf16 v[236:239], a[40:43], v[156:159], v[236:239]\n\t"
    "v_mfma_f32_16x16x32_bf16 v[236:239], a[36:39], v[252:255], v[236:239]\n\t"
    "s_waitcnt lgkmcnt(0)\n\t"
    "buffer_store_dwordx4 v[176:179], v9, s[44:47], s54 offen offset:0 nt\n\t"
    "v_mfma_f32_16x16x32_bf16 v[236:239], a[32:35], v[248:251], v[236:239]\n\t"
    "v_mfma_f32_16x16x32_bf16 v[236:239], a[28:31], v[228:231], v[236:239]\n\t"
    "v_mfma_f32_16x16x32_bf16 v[236:239], a[24:27], v[224:227], v[236:239]\n\t"
    "v_mfma_f32_16x16x32_bf16 v[240:243], a[48:51], v[224:227], 0\n\t"
    "v_cvt_pk_bf16_f32 v152, v232, v233\n\t"
    "v_cvt_pk_bf16_f32 v153, v234, v235\n\t"
    "v_mfma_f32_16x16x32_bf16 v[240:243], a[52:55], v[228:231], v[240:243]\n\t"
    "v_mfma_f32_16x16x32_bf16 v[240:243], a[56:59], v[248:251], v[240:243]\n\t"
    "v_mfma_f32_16x16x32_bf16 v[240:243], a[60:63], v[252:255], v[240:243]\n\t"
    "v_mfma_f32_16x16x32_bf16 v[240:243], a[64:67], v[156:159], v[240:243]\n\t"
    "v_mfma_f32_16x16x32_bf16 v[240:243], a[68:71], v[172:175], v[240:243]\n\t"
    "v_cvt_pk_bf16_f32 v154, v236, v237\n\t"
    "v_cvt_pk_bf16_f32 v155, v238, v239\n\t"
    "v_mfma_f32_16x16x32_bf16 v[244:247], a[92:95], v[172:175], 0\n\t"
    "v_mfma_f32_16x16x32_bf16 v[244:247], a[88:91], v[156:159], v[244:247]\n\t"
    "v_mfma_f32_16x16x32_bf16 v[244:247], a[84:87], v[252:255], v[244:247]\n\t"
    "s_add_u32 m0, s61, 20224\n\t"
    "v_mfma_f32_16x16x32_bf16 v[244:247], a[80:83], v[248:251], v[244:247]\n\t"
    "buffer_load_dwordx4 v9, s[36:39], s49 offen offset:256 nt lds\n\t"
    "v_mfma_f32_16x16x32_bf16 v[244:247], a[76:79], v[228:231], v[244:247]\n\t"
    "v_cvt_pk_bf16_f32 v168, v240, v241\n\t"
    "v_cvt_pk_bf16_f32 v169, v242, v243\n\t"
    "v_mfma_f32_16x16x32_bf16 v[244:247], a[72:75], v[224:227], v[244:247]\n\t"
    "v_mfma_f32_16x16x32_bf16 v[232:235], a[96:99], v[224:227], 0\n\t"
    "v_mfma_f32_16x16x32_bf16 v[232:235], a[100:103], v[228:231], v[232:235]\n\t"
    "ds_read_b128 v[184:187], v8 offset:8192\n\t"
    "v_mfma_f32_16x16x32_bf16 v[232:235], a[104:107], v[248:251], v[232:235]\n\t"
    "v_mfma_f32_16x16x32_bf16 v[232:235], a[108:111], v[252:255], v[232:235]\n\t"
    "v_mfma_f32_16x16x32_bf16 v[232:235], a[112:115], v[156:159], v[232:235]\n\t"
    "v_mfma_f32_16x16x32_bf16 v[232:235], a[116:119], v[172:175], v[232:235]\n\t"
    "v_mfma_f32_16x16x32_bf16 v[236:239], a[140:143], v[172:175], 0\n\t"
    "v_cvt_pk_bf16_f32 v120, v244, v245\n\t"
    "v_cvt_pk_bf16_f32 v121, v246, v247\n\t"
    "v_mfma_f32_16x16x32_bf16 v[236:239], a[136:139], v[156:159], v[236:239]\n\t"
    "ds_read_b64 v[174:175], v2 offset:6144\n\t"
    "v_mfma_f32_16x16x32_bf16 v[236:239], a[132:135], v[252:255], v[236:239]\n\t"
    "s_waitcnt lgkmcnt(1)\n\t"
    "buffer_store_dwordx4 v[184:187], v9, s[44:47], s54 offen offset:128 nt\n\t"
    "v_mfma_f32_16x16x32_bf16 v[236:239], a[128:131], v[248:251], v[236:239]\n\t"
    "v_cvt_pk_bf16_f32 v122, v232, v233\n\t"
    "v_cvt_pk_bf16_f32 v123, v234, v235\n\t"
    "v_mfma_f32_16x16x32_bf16 v[236:239], a[124:127], v[228:231], v[236:239]\n\t"
    "v_mfma_f32_16x16x32_bf16 v[236:239], a[120:123], v[224:227], v[236:239]\n\t"
    "v_mfma_f32_16x16x32_bf16 v[240:243], a[0:3], v[200:203], 0\n\t"
    "v_mfma_f32_16x16x32_bf16 v[240:243], a[4:7], v[204:207], v[240:243]\n\t"
    "v_mfma_f32_16x16x32_bf16 v[240:243], a[8:11], v[208:211], v[240:243]\n\t"
    "s_add_u32 m0, s61, 24576\n\t"
    "v_mfma_f32_16x16x32_bf16 v[240:243], a[12:15], v[212:215], v[240:243]\n\t"
    "s_add_u32 s59, s59, 16\n\t"
    "buffer_load_dwordx4 v10, s[40:43], s56 offen nt lds\n\t"
    "v_mfma_f32_16x16x32_bf16 v[240:243], a[16:19], v[216:219], v[240:243]\n\t"
    "s_cmp_ge_u32 s59, 48\n\t"
    "s_cselect_b32 s63, 48, 0\n\t"
    "s_addc_u32 s58, s58, 5\n\t"
    "v_cvt_pk_bf16_f32 v136, v236, v237\n\t"
    "v_cvt_pk_bf16_f32 v137, v238, v239\n\t"
    "v_mfma_f32_16x16x32_bf16 v[240:243], a[20:23], v[220:223], v[240:243]\n\t"
    "s_sub_u32 s59, s59, s63\n\t"
    "v_mfma_f32_16x16x32_bf16 v[244:247], a[44:47], v[220:223], 0\n\t"
    "s_lshl_b32 s64, s59, 3\n\t"
    "v_mfma_f32_16x16x32_bf16 v[244:247], a[40:43], v[216:219], v[244:247]\n\t"
    "s_lshl_b32 s63, s58, 3\n\t"
    "v_mfma_f32_16x16x32_bf16 v[244:247], a[36:39], v[212:215], v[244:247]\n\t"
    "s_add_u32 s64, s64, s66\n\t"
    "v_mfma_f32_16x16x32_bf16 v[244:247], a[32:35], v[208:211], v[244:247]\n\t"
    "s_add_u32 s63, s63, s65\n\t"
    "v_cvt_pk_bf16_f32 v156, v240, v241\n\t"
    "v_cvt_pk_bf16_f32 v157, v242, v243\n\t"
    "v_mfma_f32_16x16x32_bf16 v[244:247], a[28:31], v[204:207], v[244:247]\n\t"
    "s_cmp_eq_u32 s64, 384\n\t"
    "s_cselect_b32 s64, 0, s64\n\t"
    "v_mfma_f32_16x16x32_bf16 v[244:247], a[24:27], v[200:203], v[244:247]\n\t"
    "s_add_u32 s72, s72, 256\n\t"
    "v_mfma_f32_16x16x32_bf16 v[232:235], a[48:51], v[200:203], 0\n\t"
    "s_mul_i32 s64, s64, 384\n\t"
    "v_mfma_f32_16x16x32_bf16 v[232:235], a[52:55], v[204:207], v[232:235]\n\t"
    "s_mul_i32 s68, s63, 147456\n\t"
    "ds_read_b128 v[192:195], v8 offset:16384\n\t"
    "v_mfma_f32_16x16x32_bf16 v[232:235], a[56:59], v[208:211], v[232:235]\n\t"
    "s_add_u32 s68, s68, s64\n\t"
    "v_cvt_pk_bf16_f32 v158, v244, v245\n\t"
    "v_cvt_pk_bf16_f32 v159, v246, v247\n\t"
    "v_mfma_f32_16x16x32_bf16 v[232:235], a[60:63], v[212:215], v[232:235]\n\t"
    "s_lshl_b32 s70, s72, 13\n\t"
    "v_mfma_f32_16x16x32_bf16 v[232:235], a[64:67], v[216:219], v[232:235]\n\t"
    "s_cmp_ge_u32 s63, 380\n\t"
    "s_cselect_b32 s63, 56623104, 0\n\t"
    "v_mfma_f32_16x16x32_bf16 v[232:235], a[68:71], v[220:223], v[232:235]\n\t"
    "s_add_u32 s69, s68, 589824\n\t"
    "v_mfma_f32_16x16x32_bf16 v[236:239], a[92:95], v[220:223], 0\n\t"
    "s_add_u32 s71, s70, 4096\n\t"
    "v_mfma_f32_16x16x32_bf16 v[236:239], a[88:91], v[216:219], v[236:239]\n\t"
    "s_sub_u32 s69, s69, s63\n\t"
    "v_mfma_f32_16x16x32_bf16 v[236:239], a[84:87], v[212:215], v[236:239]\n\t"
    "s_add_u32 m0, s61, 28672\n\t"
    "s_waitcnt lgkmcnt(0)\n\t"
    "buffer_store_dwordx4 v[192:195], v9, s[44:47], s54 offen offset:256 nt\n\t"
    "v_mfma_f32_16x16x32_bf16 v[236:239], a[80:83], v[208:211], v[236:239]\n\t"
    "v_cvt_pk_bf16_f32 v172, v232, v233\n\t"
    "v_cvt_pk_bf16_f32 v173, v234, v235\n\t"
    "v_mfma_f32_16x16x32_bf16 v[236:239], a[76:79], v[204:207], v[236:239]\n\t"
    "v_mfma_f32_16x16x32_bf16 v[236:239], a[72:75], v[200:203], v[236:239]\n\t"
    "buffer_load_dwordx4 v10, s[40:43], s57 offen nt lds\n\t"
    "v_mfma_f32_16x16x32_bf16 v[240:243], a[96:99], v[200:203], 0\n\t"
    "s_cmp_lt_u32 s60, 7\n\t"
    "s_cselect_b32 s38, 56623104, 0\n\t"
    "s_cselect_b32 s42, s42, s18\n\t"
    "v_mfma_f32_16x16x32_bf16 v[240:243], a[100:103], v[204:207], v[240:243]\n\t"
    "s_cmp_lt_u32 s60, 7\n\t"
    "s_cselect_b64 s[40:41], s[40:41], s[24:25]\n\t"
    "s_cselect_b64 s[70:71], s[70:71], s[16:17]\n\t"
    "v_mfma_f32_16x16x32_bf16 v[240:243], a[104:107], v[208:211], v[240:243]\n\t"
    "v_mfma_f32_16x16x32_bf16 v[240:243], a[108:111], v[212:215], v[240:243]\n\t"
    "v_mfma_f32_16x16x32_bf16 v[240:243], a[112:115], v[216:219], v[240:243]\n\t"
    "v_cvt_pk_bf16_f32 v124, v236, v237\n\t"
    "v_cvt_pk_bf16_f32 v125, v238, v239\n\t"
    "v_mfma_f32_16x16x32_bf16 v[240:243], a[116:119], v[220:223], v[240:243]\n\t"
    "v_mfma_f32_16x16x32_bf16 v[244:247], a[140:143], v[220:223], 0\n\t"
    "v_mfma_f32_16x16x32_bf16 v[244:247], a[136:139], v[216:219], v[244:247]\n\t"
    "v_mfma_f32_16x16x32_bf16 v[244:247], a[132:135], v[212:215], v[244:247]\n\t"
    "v_mfma_f32_16x16x32_bf16 v[244:247], a[128:131], v[208:211], v[244:247]\n\t"
    "v_mfma_f32_16x16x32_bf16 v[244:247], a[124:127], v[204:207], v[244:247]\n\t"
    "v_mfma_f32_16x16x32_bf16 v[244:247], a[120:123], v[200:203], v[244:247]\n\t"
    "ENTRY_%=:\n\t"
    "v_mfma_f32_16x16x32_bf16 v[200:203], v[112:115], v[144:147], v[48:51]\n\t"
    "v_xor_b32 v2, 0x20, v2\n\t"
    "v_mfma_f32_16x16x32_bf16 v[200:203], v[128:131], v[160:163], v[200:203]\n\t"
    "ds_read_b128 v[196:199], v8 offset:4096\n\t"
    "ds_read_b64 v[162:163], v2 offset:0\n\t"
    "v_mfma_f32_16x16x32_bf16 v[204:207], v[112:115], v[148:151], v[52:55]\n\t"
    "v_mfma_f32_16x16x32_bf16 v[204:207], v[128:131], v[164:167], v[204:207]\n\t"
    "ds_read_b64 v[166:167], v2 offset:2048\n\t"
    "v_mfma_f32_16x16x32_bf16 v[208:211], v[112:115], v[152:155], v[56:59]\n\t"
    "v_cvt_pk_bf16_f32 v140, v244, v245\n\t"
    "v_cvt_pk_bf16_f32 v126, v240, v241\n\t"
    "v_mfma_f32_16x16x32_bf16 v[208:211], v[128:131], v[168:171], v[208:211]\n\t"
    "ds_read_b64 v[170:171], v2 offset:4096\n\t"
    "v_cvt_pk_bf16_f32 v127, v242, v243\n\t"
    "v_mfma_f32_16x16x32_bf16 v[212:215], v[112:115], v[156:159], v[60:63]\n\t"
    "v_cvt_pk_bf16_f32 v141, v246, v247\n\t"
    "v_max3_f32 v11, v200, v201, v202\n\t"
    "v_mfma_f32_16x16x32_bf16 v[212:215], v[128:131], v[172:175], v[212:215]\n\t"
    "ds_read_b64 v[174:175], v2 offset:6144\n\t"
    "v_xor_b32 v2, 0x40, v2\n\t"
    "v_mfma_f32_16x16x32_bf16 v[216:219], v[116:119], v[144:147], v[64:67]\n\t"
    "s_waitcnt lgkmcnt(3)\n\t"
    "buffer_store_dwordx4 v[196:199], v9, s[44:47], s55 offen offset:0 nt\n\t"
    "v_mfma_f32_16x16x32_bf16 v[216:219], v[132:135], v[160:163], v[216:219]\n\t"
    "ds_read_b64 v[162:163], v2 offset:0\n\t"
    "v_max3_f32 v128, v204, v205, v206\n\t"
    "v_mfma_f32_16x16x32_bf16 v[220:223], v[116:119], v[148:151], v[68:71]\n\t"
    "s_waitcnt lgkmcnt(3)\n\t"
    "v_max3_f32 v129, v208, v209, v210\n\t"
    "v_max_f32 v11, v11, v203\n\t"
    "v_mfma_f32_16x16x32_bf16 v[220:223], v[132:135], v[164:167], v[220:223]\n\t"
    "ds_read_b64 v[166:167], v2 offset:2048\n\t"
    "v_max_f32 v128, v128, v207\n\t"
    "v_mfma_f32_16x16x32_bf16 v[224:227], v[116:119], v[152:155], v[72:75]\n\t"
    "s_waitcnt lgkmcnt(3)\n\t"
    "v_max_f32 v129, v129, v211\n\t"
    "v_max3_f32 v11, v11, v216, v217\n\t"
    "v_mfma_f32_16x16x32_bf16 v[224:227], v[132:135], v[168:171], v[224:227]\n\t"
    "ds_read_b64 v[170:171], v2 offset:4096\n\t"
    "v_max3_f32 v11, v11, v218, v219\n\t"
    "v_mfma_f32_16x16x32_bf16 v[228:231], v[116:119], v[156:159], v[76:79]\n\t"
    "s_waitcnt lgkmcnt(3)\n\t"
    "v_max3_f32 v128, v128, v220, v221\n\t"
    "v_max3_f32 v128, v128, v222, v223\n\t"
    "v_mfma_f32_16x16x32_bf16 v[228:231], v[132:135], v[172:175], v[228:231]\n\t"
    "ds_read_b64 v[174:175], v2 offset:6144\n\t"
    "v_xor_b32 v2, 0x20, v2\n\t"
    "v_mfma_f32_16x16x32_bf16 v[232:235], v[124:127], v[144:147], v[96:99]\n\t"
    "s_waitcnt lgkmcnt(3)\n\t"
    "v_max3_f32 v132, v212, v213, v214\n\t"
    "v_max_f32 v132, v132, v215\n\t"
    "v_mfma_f32_16x16x32_bf16 v[232:235], v[140:143], v[160:163], v[232:235]\n\t"
    "ds_read_b64 v[162:163], v2 offset:0\n\t"
    "v_max3_f32 v129, v129, v224, v225\n\t"
    "v_mfma_f32_16x16x32_bf16 v[236:239], v[124:127], v[148:151], v[100:103]\n\t"
    "s_waitcnt lgkmcnt(3)\n\t"
    "v_max3_f32 v132, v132, v228, v229\n\t"
    "v_max3_f32 v129, v129, v226, v227\n\t"
    "v_mfma_f32_16x16x32_bf16 v[236:239], v[140:143], v[164:167], v[236:239]\n\t"
    "ds_read_b64 v[166:167], v2 offset:2048\n\t"
    "v_max3_f32 v132, v132, v230, v231\n\t"
    "v_mfma_f32_16x16x32_bf16 v[240:243], v[124:127], v[152:155], v[104:107]\n\t"
    "s_waitcnt lgkmcnt(3)\n\t"
    "v_max3_f32 v11, v11, v232, v233\n\t"
    "v_max3_f32 v11, v11, v234, v235\n\t"
    "v_mfma_f32_16x16x32_bf16 v[240:243], v[140:143], v[168:171], v[240:243]\n\t"
    "ds_read_b64 v[170:171], v2 offset:4096\n\t"
    "v_mfma_f32_16x16x32_bf16 v[244:247], v[124:127], v[156:159], v[108:111]\n\t"
    "s_waitcnt lgkmcnt(3)\n\t"
    "v_max3_f32 v128, v128, v236, v237\n\t"
    "v_max3_f32 v128, v128, v238, v239\n\t"
    "v_mfma_f32_16x16x32_bf16 v[244:247], v[140:143], v[172:175], v[244:247]\n\t"
    "ds_read_b64 v[174:175], v2 offset:6144\n\t"
    "v_xor_b32 v2, 0x8040, v2\n\t"
    "v_mfma_f32_16x16x32_bf16 v[248:251], v[120:123], v[144:147], v[80:83]\n\t"
    "s_waitcnt lgkmcnt(3)\n\t"
    "ds_read_b128 v[144:147], v0 offset:0\n\t"
    "v_max3_f32 v129, v129, v240, v241\n\t"
    "v_mfma_f32_16x16x32_bf16 v[248:251], v[136:139], v[160:163], v[248:251]\n\t"
    "ds_read_b128 v[160:163], v1 offset:0\n\t"
    "v_max3_f32 v129, v129, v242, v243\n\t"
    "v_mfma_f32_16x16x32_bf16 v[112:115], v[120:123], v[148:151], v[84:87]\n\t"
    "s_waitcnt lgkmcnt(4)\n\t"
    "ds_read_b128 v[148:151], v0 offset:8192\n\t"
    "v_max3_f32 v132, v132, v244, v245\n\t"
    "v_mfma_f32_16x16x32_bf16 v[112:115], v[136:139], v[164:167], v[112:115]\n\t"
    "ds_read_b128 v[180:183], v8 offset:12288\n\t"
    "ds_read_b128 v[164:167], v1 offset:8192\n\t"
    "v_mfma_f32_16x16x32_bf16 v[116:119], v[120:123], v[152:155], v[88:91]\n\t"
    "s_waitcnt lgkmcnt(6)\n\t"
    "ds_read_b128 v[152:155], v0 offset:16384\n\t"
    "v_max3_f32 v132, v132, v246, v247\n\t"
    "v_mfma_f32_16x16x32_bf16 v[116:119], v[136:139], v[168:171], v[116:119]\n\t"
    "ds_read_b128 v[168:171], v1 offset:16384\n\t"
    "v_max3_f32 v11, v11, v248, v249\n\t"
    "v_mfma_f32_16x16x32_bf16 v[124:127], v[120:123], v[156:159], v[92:95]\n\t"
    "s_waitcnt lgkmcnt(7)\n\t"
    "v_max3_f32 v128, v128, v112, v113\n\t"
    "v_max3_f32 v11, v11, v250, v251\n\t"
    "v_mfma_f32_16x16x32_bf16 v[124:127], v[136:139], v[172:175], v[124:127]\n\t"
    "s_waitcnt lgkmcnt(6)\n\t"
    "v_max3_f32 v128, v128, v114, v115\n\t"
    "v_mfma_f32_16x16x32_bf16 v[120:123], v[144:147], a[192:195], 0\n\t"
    "s_waitcnt lgkmcnt(5)\n\t"
    "v_max3_f32 v129, v129, v116, v117\n\t"
    "v_max3_f32 v129, v129, v118, v119\n\t"
    "v_mfma_f32_16x16x32_bf16 v[120:123], v[160:163], a[196:199], v[120:123]\n\t"
    "s_waitcnt lgkmcnt(4)\n\t"
    "v_permlane32_swap_b32 v11, v128\n\t"
    "v_mfma_f32_16x16x32_bf16 v[120:123], v[148:151], a[200:203], v[120:123]\n\t"
    "s_waitcnt lgkmcnt(2)\n\t"
    "buffer_store_dwordx4 v[180:183], v9, s[44:47], s55 offen offset:128 nt\n\t"
    "v_mfma_f32_16x16x32_bf16 v[120:123], v[164:167], a[204:207], v[120:123]\n\t"
    "s_waitcnt lgkmcnt(1)\n\t"
    "v_max3_f32 v132, v132, v124, v125\n\t"
    "v_max3_f32 v132, v132, v126, v127\n\t"
    "v_mfma_f32_16x16x32_bf16 v[120:123], v[152:155], a[208:211], v[120:123]\n\t"
    "s_waitcnt lgkmcnt(0)\n\t"
    "v_max_f32 v11, v11, v128\n\t"
    "v_mfma_f32_16x16x32_bf16 v[120:123], v[168:171], a[212:215], v[120:123]\n\t"
    "v_permlane32_swap_b32 v129, v132\n\t"
    "v_mfma_f32_16x16x32_bf16 v[156:159], v[168:171], a[164:167], 0\n\t"
    "v_max_f32 v129, v129, v132\n\t"
    "v_mfma_f32_16x16x32_bf16 v[156:159], v[152:155], a[160:163], v[156:159]\n\t"
    "v_mfma_f32_16x16x32_bf16 v[156:159], v[164:167], a[156:159], v[156:159]\n\t"
    "v_permlane16_swap_b32 v11, v129\n\t"
    "v_mfma_f32_16x16x32_bf16 v[156:159], v[148:151], a[152:155], v[156:159]\n\t"
    "v_max_f32 v11, v11, v129\n\t"
    "v_cvt_pk_bf16_f32 v192, v120, v121\n\t"
    "v_mfma_f32_16x16x32_bf16 v[156:159], v[160:163], a[148:151], v[156:159]\n\t"
    "v_cvt_pk_bf16_f32 v193, v122, v123\n\t"
    "v_mul_f32 v11, 0xbe553b95, v11\n\t"
    "v_mfma_f32_16x16x32_bf16 v[156:159], v[144:147], a[144:147], v[156:159]\n\t"
    "v_mov_b32 v129, v11\n\t"
    "v_mfma_f32_16x16x32_bf16 v[252:255], v[144:147], a[168:171], 0\n\t"
    "ds_read_b128 v[144:147], v0 offset:2048\n\t"
    "v_mfma_f32_16x16x32_bf16 v[252:255], v[160:163], a[172:175], v[252:255]\n\t"
    "ds_read_b128 v[160:163], v1 offset:2048\n\t"
    "v_mfma_f32_16x16x32_bf16 v[252:255], v[148:151], a[176:179], v[252:255]\n\t"
    "ds_read_b128 v[148:151], v0 offset:10240\n\t"
    "v_mfma_f32_16x16x32_bf16 v[252:255], v[164:167], a[180:183], v[252:255]\n\t"
    "ds_read_b128 v[164:167], v1 offset:10240\n\t"
    "v_cvt_pk_bf16_f32 v176, v156, v157\n\t"
    "v_mfma_f32_16x16x32_bf16 v[252:255], v[152:155], a[184:187], v[252:255]\n\t"
    "ds_read_b128 v[152:155], v0 offset:18432\n\t"
    "v_cvt_pk_bf16_f32 v177, v158, v159\n\t"
    "v_mfma_f32_16x16x32_bf16 v[252:255], v[168:171], a[188:191], v[252:255]\n\t"
    "ds_read_b128 v[168:171], v1 offset:18432\n\t"
    "v_permlane16_swap_b32 v11, v129\n\t"
    "v_mov_b32 v128, v11\n\t"
    "v_mov_b32 v132, v129\n\t"
    "s_waitcnt lgkmcnt(5)\n\t"
    "v_mfma_f32_16x16x32_bf16 v[120:123], v[144:147], a[192:195], 0\n\t"
    "s_waitcnt lgkmcnt(4)\n\t"
    "v_permlane32_swap_b32 v11, v128\n\t"
    "v_mfma_f32_16x16x32_bf16 v[120:123], v[160:163], a[196:199], v[120:123]\n\t"
    "s_waitcnt lgkmcnt(3)\n\t"
    "v_fma_f32 v200, v200, s62, v11\n\t"
    "v_cvt_pk_bf16_f32 v184, v252, v253\n\t"
    "v_mfma_f32_16x16x32_bf16 v[120:123], v[148:151], a[200:203], v[120:123]\n\t"
    "s_waitcnt lgkmcnt(2)\n\t"
    "v_cvt_pk_bf16_f32 v185, v254, v255\n\t"
    "v_fma_f32 v201, v201, s62, v11\n\t"
    "v_mfma_f32_16x16x32_bf16 v[120:123], v[164:167], a[204:207], v[120:123]\n\t"
    "s_waitcnt lgkmcnt(1)\n\t"
    "ds_read_b128 v[188:191], v8 offset:20480\n\t"
    "v_fma_f32 v202, v202, s62, v11\n\t"
    "v_mfma_f32_16x16x32_bf16 v[120:123], v[152:155], a[208:211], v[120:123]\n\t"
    "s_waitcnt lgkmcnt(1)\n\t"
    "v_exp_f32 v200, v200\n\t"
    "v_mfma_f32_16x16x32_bf16 v[120:123], v[168:171], a[212:215], v[120:123]\n\t"
    "v_exp_f32 v201, v201\n\t"
    "v_mfma_f32_16x16x32_bf16 v[156:159], v[168:171], a[164:167], 0\n\t"
    "v_fma_f32 v203, v203, s62, v11\n\t"
    "v_fma_f32 v216, v216, s62, v11\n\t"
    "v_mfma_f32_16x16x32_bf16 v[156:159], v[152:155], a[160:163], v[156:159]\n\t"
    "v_exp_f32 v202, v202\n\t"
    "v_mfma_f32_16x16x32_bf16 v[156:159], v[164:167], a[156:159], v[156:159]\n\t"
    "v_exp_f32 v203, v203\n\t"
    "v_mfma_f32_16x16x32_bf16 v[156:159], v[148:151], a[152:155], v[156:159]\n\t"
    "v_cvt_pk_bf16_f32 v194, v120, v121\n\t"
    "v_cvt_pk_bf16_f32 v195, v122, v123\n\t"
    "v_mfma_f32_16x16x32_bf16 v[156:159], v[160:163], a[148:151], v[156:159]\n\t"
    "s_waitcnt lgkmcnt(0)\n\t"
    "buffer_store_dwordx4 v[188:191], v9, s[44:47], s55 offen offset:256 nt\n\t"
    "v_mfma_f32_16x16x32_bf16 v[156:159], v[144:147], a[144:147], v[156:159]\n\t"
    "v_fma_f32 v217, v217, s62, v11\n\t"
    "v_fma_f32 v218, v218, s62, v11\n\t"
    "v_mfma_f32_16x16x32_bf16 v[252:255], v[144:147], a[168:171], 0\n\t"
    "ds_read_b128 v[144:147], v0 offset:4096\n\t"
    "v_fma_f32 v219, v219, s62, v11\n\t"
    "v_mfma_f32_16x16x32_bf16 v[252:255], v[160:163], a[172:175], v[252:255]\n\t"
    "ds_read_b128 v[160:163], v1 offset:4096\n\t"
    "v_cvt_pk_bf16_f32 v200, v200, v201\n\t"
    "v_mfma_f32_16x16x32_bf16 v[252:255], v[148:151], a[176:179], v[252:255]\n\t"
    "ds_read_b128 v[148:151], v0 offset:12288\n\t"
    "v_cvt_pk_bf16_f32 v178, v156, v157\n\t"
    "v_mfma_f32_16x16x32_bf16 v[252:255], v[164:167], a[180:183], v[252:255]\n\t"
    "ds_read_b128 v[164:167], v1 offset:12288\n\t"
    "v_cvt_pk_bf16_f32 v179, v158, v159\n\t"
    "v_mfma_f32_16x16x32_bf16 v[252:255], v[152:155], a[184:187], v[252:255]\n\t"
    "ds_read_b128 v[152:155], v0 offset:20480\n\t"
    "v_cvt_pk_bf16_f32 v201, v202, v203\n\t"
    "v_mfma_f32_16x16x32_bf16 v[252:255], v[168:171], a[188:191], v[252:255]\n\t"
    "ds_read_b128 v[168:171], v1 offset:20480\n\t"
    "v_fma_f32 v248, v248, s62, v11\n\t"
    "v_exp_f32 v216, v216\n\t"
    "v_exp_f32 v217, v217\n\t"
    "v_fma_f32 v249, v249, s62, v11\n\t"
    "s_waitcnt lgkmcnt(5)\n\t"
    "v_mfma_f32_16x16x32_bf16 v[120:123], v[144:147], a[192:195], 0\n\t"
    "s_waitcnt lgkmcnt(4)\n\t"
    "v_exp_f32 v218, v218\n\t"
    "v_mfma_f32_16x16x32_bf16 v[120:123], v[160:163], a[196:199], v[120:123]\n\t"
    "s_waitcnt lgkmcnt(3)\n\t"
    "v_cvt_pk_bf16_f32 v186, v252, v253\n\t"
    "v_cvt_pk_bf16_f32 v187, v254, v255\n\t"
    "v_mfma_f32_16x16x32_bf16 v[120:123], v[148:151], a[200:203], v[120:123]\n\t"
    "s_waitcnt lgkmcnt(2)\n\t"
    "v_exp_f32 v219, v219\n\t"
    "v_mfma_f32_16x16x32_bf16 v[120:123], v[164:167], a[204:207], v[120:123]\n\t"
    "s_waitcnt lgkmcnt(1)\n\t"
    "v_cvt_pk_bf16_f32 v202, v216, v217\n\t"
    "v_cvt_pk_bf16_f32 v203, v218, v219\n\t"
    "v_mfma_f32_16x16x32_bf16 v[120:123], v[152:155], a[208:211], v[120:123]\n\t"
    "s_waitcnt lgkmcnt(0)\n\t"
    "v_exp_f32 v248, v248\n\t"
    "v_mfma_f32_16x16x32_bf16 v[120:123], v[168:171], a[212:215], v[120:123]\n\t"
    "v_exp_f32 v249, v249\n\t"
    "v_mfma_f32_16x16x32_bf16 v[156:159], v[168:171], a[164:167], 0\n\t"
    "v_fma_f32 v250, v250, s62, v11\n\t"
    "v_fma_f32 v251, v251, s62, v11\n\t"
    "v_mfma_f32_16x16x32_bf16 v[156:159], v[152:155], a[160:163], v[156:159]\n\t"
    "v_exp_f32 v250, v250\n\t"
    "v_mfma_f32_16x16x32_bf16 v[156:159], v[164:167], a[156:159], v[156:159]\n\t"
    "v_exp_f32 v251, v251\n\t"
    "v_mfma_f32_16x16x32_bf16 v[156:159], v[148:151], a[152:155], v[156:159]\n\t"
    "v_cvt_pk_bf16_f32 v196, v120, v121\n\t"
    "v_cvt_pk_bf16_f32 v197, v122, v123\n\t"
    "v_mfma_f32_16x16x32_bf16 v[156:159], v[160:163], a[148:151], v[156:159]\n\t"
    "v_fma_f32 v232, v232, s62, v11\n\t"
    "v_fma_f32 v233, v233, s62, v11\n\t"
    "v_mfma_f32_16x16x32_bf16 v[156:159], v[144:147], a[144:147], v[156:159]\n\t"
    "v_fma_f32 v234, v234, s62, v11\n\t"
    "v_fma_f32 v235, v235, s62, v11\n\t"
    "v_mfma_f32_16x16x32_bf16 v[252:255], v[144:147], a[168:171], 0\n\t"
    "ds_read_b128 v[144:147], v0 offset:6144\n\t"
    "v_cvt_pk_bf16_f32 v248, v248, v249\n\t"
    "v_mfma_f32_16x16x32_bf16 v[252:255], v[160:163], a[172:175], v[252:255]\n\t"
    "ds_read_b128 v[160:163], v1 offset:6144\n\t"
    "v_cvt_pk_bf16_f32 v249, v250, v251\n\t"
    "v_mfma_f32_16x16x32_bf16 v[252:255], v[148:151], a[176:179], v[252:255]\n\t"
    "ds_read_b128 v[148:151], v0 offset:14336\n\t"
    "v_cvt_pk_bf16_f32 v180, v156, v157\n\t"
    "v_mfma_f32_16x16x32_bf16 v[252:255], v[164:167], a[180:183], v[252:255]\n\t"
    "ds_read_b128 v[164:167], v1 offset:14336\n\t"
    "v_cvt_pk_bf16_f32 v181, v158, v159\n\t"
    "v_mfma_f32_16x16x32_bf16 v[252:255], v[152:155], a[184:187], v[252:255]\n\t"
    "ds_read_b128 v[152:155], v0 offset:22528\n\t"
    "v_xor_b32 v0, 0x8000, v0\n\t"
    "v_mfma_f32_16x16x32_bf16 v[252:255], v[168:171], a[188:191], v[252:255]\n\t"
    "ds_read_b128 v[168:171], v1 offset:22528\n\t"
    "v_xor_b32 v1, 0x8000, v1\n\t"
    "v_exp_f32 v232, v232\n\t"
    "v_exp_f32 v233, v233\n\t"
    "v_fma_f32 v204, v204, s62, v128\n\t"
    "s_waitcnt lgkmcnt(5)\n\t"
    "v_mfma_f32_16x16x32_bf16 v[120:123], v[144:147], a[192:195], 0\n\t"
    "s_waitcnt lgkmcnt(4)\n\t"
    "v_exp_f32 v234, v234\n\t"
    "v_mfma_f32_16x16x32_bf16 v[120:123], v[160:163], a[196:199], v[120:123]\n\t"
    "s_waitcnt lgkmcnt(3)\n\t"
    "v_cvt_pk_bf16_f32 v188, v252, v253\n\t"
    "v_cvt_pk_bf16_f32 v189, v254, v255\n\t"
    "v_mfma_f32_16x16x32_bf16 v[120:123], v[148:151], a[200:203], v[120:123]\n\t"
    "s_waitcnt lgkmcnt(2)\n\t"
    "v_exp_f32 v235, v235\n\t"
    "v_mfma_f32_16x16x32_bf16 v[120:123], v[164:167], a[204:207], v[120:123]\n\t"
    "s_waitcnt lgkmcnt(1)\n\t"
    "v_cvt_pk_bf16_f32 v250, v232, v233\n\t"
    "v_cvt_pk_bf16_f32 v251, v234, v235\n\t"
    "v_mfma_f32_16x16x32_bf16 v[120:123], v[152:155], a[208:211], v[120:123]\n\t"
    "s_waitcnt lgkmcnt(0)\n\t"
    "v_fma_f32 v205, v205, s62, v128\n\t"
    "v_fma_f32 v206, v206, s62, v128\n\t"
    "v_mfma_f32_16x16x32_bf16 v[120:123], v[168:171], a[212:215], v[120:123]\n\t"
    "v_exp_f32 v204, v204\n\t"
    "v_mfma_f32_16x16x32_bf16 v[156:159], v[168:171], a[164:167], 0\n\t"
    "v_exp_f32 v205, v205\n\t"
    "v_mfma_f32_16x16x32_bf16 v[156:159], v[152:155], a[160:163], v[156:159]\n\t"
    "v_fma_f32 v207, v207, s62, v128\n\t"
    "v_fma_f32 v220, v220, s62, v128\n\t"
    "v_mfma_f32_16x16x32_bf16 v[156:159], v[164:167], a[156:159], v[156:159]\n\t"
    "v_exp_f32 v206, v206\n\t"
    "v_mfma_f32_16x16x32_bf16 v[156:159], v[148:151], a[152:155], v[156:159]\n\t"
    "v_cvt_pk_bf16_f32 v198, v120, v121\n\t"
    "v_cvt_pk_bf16_f32 v199, v122, v123\n\t"
    "v_mfma_f32_16x16x32_bf16 v[156:159], v[160:163], a[148:151], v[156:159]\n\t"
    "v_exp_f32 v207, v207\n\t"
    "v_mfma_f32_16x16x32_bf16 v[156:159], v[144:147], a[144:147], v[156:159]\n\t"
    "v_fma_f32 v221, v221, s62, v128\n\t"
    "v_fma_f32 v222, v222, s62, v128\n\t"
    "v_mfma_f32_16x16x32_bf16 v[252:255], v[144:147], a[168:171], 0\n\t"
    "ds_read_b128 v[144:147], v3 offset:0\n\t"
    "v_fma_f32 v223, v223, s62, v128\n\t"
    "v_mfma_f32_16x16x32_bf16 v[252:255], v[160:163], a[172:175], v[252:255]\n\t"
    "ds_read_b128 v[160:163], v3 offset:1024\n\t"
    "v_cvt_pk_bf16_f32 v204, v204, v205\n\t"
    "v_mfma_f32_16x16x32_bf16 v[252:255], v[148:151], a[176:179], v[252:255]\n\t"
    "ds_read_b128 v[148:151], v3 offset:2048\n\t"
    "v_cvt_pk_bf16_f32 v182, v156, v157\n\t"
    "v_mfma_f32_16x16x32_bf16 v[252:255], v[164:167], a[180:183], v[252:255]\n\t"
    "ds_read_b128 v[164:167], v3 offset:3072\n\t"
    "v_cvt_pk_bf16_f32 v183, v158, v159\n\t"
    "v_mfma_f32_16x16x32_bf16 v[252:255], v[152:155], a[184:187], v[252:255]\n\t"
    "ds_read_b128 v[152:155], v3 offset:4096\n\t"
    "v_cvt_pk_bf16_f32 v205, v206, v207\n\t"
    "v_mfma_f32_16x16x32_bf16 v[252:255], v[168:171], a[188:191], v[252:255]\n\t"
    "ds_read_b128 v[168:171], v3 offset:5120\n\t"
    "v_fma_f32 v112, v112, s62, v128\n\t"
    "v_exp_f32 v220, v220\n\t"
    "v_exp_f32 v221, v221\n\t"
    "v_fma_f32 v113, v113, s62, v128\n\t"
    "s_waitcnt lgkmcnt(5)\n\t"
    "v_mfma_f32_16x16x32_bf16 v[120:123], v[24:27], v[144:147], 0\n\t"
    "s_waitcnt lgkmcnt(4)\n\t"
    "v_exp_f32 v222, v222\n\t"
    "v_mfma_f32_16x16x32_bf16 v[120:123], v[28:31], v[160:163], v[120:123]\n\t"
    "s_waitcnt lgkmcnt(3)\n\t"
    "v_cvt_pk_bf16_f32 v190, v252, v253\n\t"
    "v_cvt_pk_bf16_f32 v191, v254, v255\n\t"
    "v_mfma_f32_16x16x32_bf16 v[120:123], v[32:35], v[148:151], v[120:123]\n\t"
    "s_waitcnt lgkmcnt(2)\n\t"
    "v_exp_f32 v223, v223\n\t"
    "v_mfma_f32_16x16x32_bf16 v[120:123], v[36:39], v[164:167], v[120:123]\n\t"
    "s_waitcnt lgkmcnt(1)\n\t"
    "v_cvt_pk_bf16_f32 v206, v220, v221\n\t"
    "v_cvt_pk_bf16_f32 v207, v222, v223\n\t"
    "v_mfma_f32_16x16x32_bf16 v[120:123], v[40:43], v[152:155], v[120:123]\n\t"
    "s_waitcnt lgkmcnt(0)\n\t"
    "v_exp_f32 v112, v112\n\t"
    "v_mfma_f32_16x16x32_bf16 v[120:123], v[44:47], v[168:171], v[120:123]\n\t"
    "v_exp_f32 v113, v113\n\t"
    "v_mfma_f32_16x16x32_bf16 v[156:159], a[236:239], v[168:171], 0\n\t"
    "v_fma_f32 v114, v114, s62, v128\n\t"
    "v_fma_f32 v115, v115, s62, v128\n\t"
    "v_mfma_f32_16x16x32_bf16 v[156:159], a[232:235], v[152:155], v[156:159]\n\t"
    "v_exp_f32 v114, v114\n\t"
    "v_mfma_f32_16x16x32_bf16 v[156:159], a[228:231], v[164:167], v[156:159]\n\t"
    "v_exp_f32 v115, v115\n\t"
    "v_mfma_f32_16x16x32_bf16 v[156:159], a[224:227], v[148:151], v[156:159]\n\t"
    "v_cvt_pk_bf16_f32 v120, v120, v121\n\t"
    "v_cvt_pk_bf16_f32 v121, v122, v123\n\t"
    "v_mfma_f32_16x16x32_bf16 v[156:159], a[220:223], v[160:163], v[156:159]\n\t"
    "ds_write_b64 v7, v[120:121] offset:0\n\t"
    "v_mfma_f32_16x16x32_bf16 v[156:159], a[216:219], v[144:147], v[156:159]\n\t"
    "v_fma_f32 v236, v236, s62, v128\n\t"
    "v_fma_f32 v237, v237, s62, v128\n\t"
    "v_mfma_f32_16x16x32_bf16 v[252:255], a[240:243], v[144:147], 0\n\t"
    "ds_read_b128 v[144:147], v3 offset:6144\n\t"
    "v_fma_f32 v238, v238, s62, v128\n\t"
    "v_mfma_f32_16x16x32_bf16 v[252:255], a[244:247], v[160:163], v[252:255]\n\t"
    "ds_read_b128 v[160:163], v3 offset:7168\n\t"
    "v_fma_f32 v239, v239, s62, v128\n\t"
    "v_mfma_f32_16x16x32_bf16 v[252:255], a[248:251], v[148:151], v[252:255]\n\t"
    "ds_read_b128 v[148:151], v3 offset:8192\n\t"
    "v_cvt_pk_bf16_f32 v156, v156, v157\n\t"
    "v_mfma_f32_16x16x32_bf16 v[252:255], a[252:255], v[164:167], v[252:255]\n\t"
    "ds_read_b128 v[164:167], v3 offset:9216\n\t"
    "v_cvt_pk_bf16_f32 v157, v158, v159\n\t"
    "v_mfma_f32_16x16x32_bf16 v[252:255], v[16:19], v[152:155], v[252:255]\n\t"
    "ds_read_b128 v[152:155], v3 offset:10240\n\t"
    "v_cvt_pk_bf16_f32 v112, v112, v113\n\t"
    "v_mfma_f32_16x16x32_bf16 v[252:255], v[20:23], v[168:171], v[252:255]\n\t"
    "ds_read_b128 v[168:171], v3 offset:11264\n\t"
    "v_cvt_pk_bf16_f32 v113, v114, v115\n\t"
    "v_mfma_f32_16x16x32_bf16 v[216:219], v[12:15], v[200:203], 0\n\t"
    "v_exp_f32 v236, v236\n\t"
    "v_mfma_f32_16x16x32_bf16 v[216:219], v[12:15], v[248:251], v[216:219]\n\t"
    "v_exp_f32 v237, v237\n\t"
    "v_mfma_f32_16x16x32_bf16 v[232:235], v[176:179], v[200:203], 0\n\t"
    "v_exp_f32 v238, v238\n\t"
    "v_mfma_f32_16x16x32_bf16 v[232:235], v[180:183], v[248:251], v[232:235]\n\t"
    "v_cvt_pk_bf16_f32 v158, v252, v253\n\t"
    "v_cvt_pk_bf16_f32 v159, v254, v255\n\t"
    "v_mfma_f32_16x16x32_bf16 v[172:175], v[184:187], v[200:203], 0\n\t"
    "ds_write_b128 v6, v[156:159] offset:0\n\t"
    "v_mfma_f32_16x16x32_bf16 v[172:175], v[188:191], v[248:251], v[172:175]\n\t"
    "v_rcp_f32 v136, v216\n\t"
    "v_mfma_f32_16x16x32_bf16 v[216:219], v[192:195], v[200:203], 0\n\t"
    "v_exp_f32 v239, v239\n\t"
    "v_mfma_f32_16x16x32_bf16 v[216:219], v[196:199], v[248:251], v[216:219]\n\t"
    "s_waitcnt lgkmcnt(6)\n\t"
    "v_cvt_pk_bf16_f32 v114, v236, v237\n\t"
    "v_cvt_pk_bf16_f32 v115, v238, v239\n\t"
    "v_mfma_f32_16x16x32_bf16 v[120:123], v[24:27], v[144:147], 0\n\t"
    "s_waitcnt lgkmcnt(5)\n\t"
    "v_permlane32_swap_b32 v129, v132\n\t"
    "v_mfma_f32_16x16x32_bf16 v[120:123], v[28:31], v[160:163], v[120:123]\n\t"
    "s_waitcnt lgkmcnt(4)\n\t"
    "v_fma_f32 v208, v208, s62, v129\n\t"
    "v_fma_f32 v209, v209, s62, v129\n\t"
    "v_mfma_f32_16x16x32_bf16 v[120:123], v[32:35], v[148:151], v[120:123]\n\t"
    "s_waitcnt lgkmcnt(3)\n\t"
    "v_exp_f32 v208, v208\n\t"
    "v_mfma_f32_16x16x32_bf16 v[120:123], v[36:39], v[164:167], v[120:123]\n\t"
    "s_waitcnt lgkmcnt(2)\n\t"
    "v_exp_f32 v209, v209\n\t"
    "v_mfma_f32_16x16x32_bf16 v[120:123], v[40:43], v[152:155], v[120:123]\n\t"
    "s_waitcnt lgkmcnt(1)\n\t"
    "v_fma_f32 v210, v210, s62, v129\n\t"
    "v_fma_f32 v211, v211, s62, v129\n\t"
    "v_mfma_f32_16x16x32_bf16 v[120:123], v[44:47], v[168:171], v[120:123]\n\t"
    "v_exp_f32 v210, v210\n\t"
    "v_mfma_f32_16x16x32_bf16 v[156:159], a[236:239], v[168:171], 0\n\t"
    "v_exp_f32 v211, v211\n\t"
    "v_mfma_f32_16x16x32_bf16 v[156:159], a[232:235], v[152:155], v[156:159]\n\t"
    "v_fma_f32 v224, v224, s62, v129\n\t"
    "v_fma_f32 v225, v225, s62, v129\n\t"
    "v_mfma_f32_16x16x32_bf16 v[156:159], a[228:231], v[164:167], v[156:159]\n\t"
    "v_fma_f32 v226, v226, s62, v129\n\t"
    "v_cvt_pk_bf16_f32 v120, v120, v121\n\t"
    "v_mfma_f32_16x16x32_bf16 v[156:159], a[224:227], v[148:151], v[156:159]\n\t"
    "v_cvt_pk_bf16_f32 v121, v122, v123\n\t"
    "v_fma_f32 v227, v227, s62, v129\n\t"
    "v_mfma_f32_16x16x32_bf16 v[156:159], a[220:223], v[160:163], v[156:159]\n\t"
    "ds_write_b64 v7, v[120:121] offset:2048\n\t"
    "v_mfma_f32_16x16x32_bf16 v[156:159], a[216:219], v[144:147], v[156:159]\n\t"
    "v_cvt_pk_bf16_f32 v208, v208, v209\n\t"
    "v_cvt_pk_bf16_f32 v209, v210, v211\n\t"
    "v_mfma_f32_16x16x32_bf16 v[252:255], a[240:243], v[144:147], 0\n\t"
    "ds_read_b128 v[144:147], v3 offset:12288\n\t"
    "v_fma_f32 v116, v116, s62, v129\n\t"
    "v_mfma_f32_16x16x32_bf16 v[252:255], a[244:247], v[160:163], v[252:255]\n\t"
    "ds_read_b128 v[160:163], v3 offset:13312\n\t"
    "v_fma_f32 v117, v117, s62, v129\n\t"
    "v_mfma_f32_16x16x32_bf16 v[252:255], a[248:251], v[148:151], v[252:255]\n\t"
    "ds_read_b128 v[148:151], v3 offset:14336\n\t"
    "v_cvt_pk_bf16_f32 v156, v156, v157\n\t"
    "v_mfma_f32_16x16x32_bf16 v[252:255], a[252:255], v[164:167], v[252:255]\n\t"
    "ds_read_b128 v[164:167], v3 offset:15360\n\t"
    "v_cvt_pk_bf16_f32 v157, v158, v159\n\t"
    "v_mfma_f32_16x16x32_bf16 v[252:255], v[16:19], v[152:155], v[252:255]\n\t"
    "ds_read_b128 v[152:155], v3 offset:16384\n\t"
    "v_fma_f32 v118, v118, s62, v129\n\t"
    "v_mfma_f32_16x16x32_bf16 v[252:255], v[20:23], v[168:171], v[252:255]\n\t"
    "ds_read_b128 v[168:171], v3 offset:17408\n\t"
    "v_fma_f32 v119, v119, s62, v129\n\t"
    "v_mfma_f32_16x16x32_bf16 v[220:223], v[12:15], v[204:207], 0\n\t"
    "v_exp_f32 v224, v224\n\t"
    "v_mfma_f32_16x16x32_bf16 v[220:223], v[12:15], v[112:115], v[220:223]\n\t"
    "v_exp_f32 v225, v225\n\t"
    "v_mfma_f32_16x16x32_bf16 v[236:239], v[176:179], v[204:207], 0\n\t"
    "v_exp_f32 v226, v226\n\t"
    "v_mfma_f32_16x16x32_bf16 v[236:239], v[180:183], v[112:115], v[236:239]\n\t"
    "v_cvt_pk_bf16_f32 v158, v252, v253\n\t"
    "v_cvt_pk_bf16_f32 v159, v254, v255\n\t"
    "v_mfma_f32_16x16x32_bf16 v[200:203], v[184:187], v[204:207], 0\n\t"
    "ds_write_b128 v6, v[156:159] offset:2048\n\t"
    "v_mfma_f32_16x16x32_bf16 v[200:203], v[188:191], v[112:115], v[200:203]\n\t"
    "v_rcp_f32 v133, v220\n\t"
    "v_mfma_f32_16x16x32_bf16 v[248:251], v[192:195], v[204:207], 0\n\t"
    "v_exp_f32 v227, v227\n\t"
    "v_mfma_f32_16x16x32_bf16 v[248:251], v[196:199], v[112:115], v[248:251]\n\t"
    "s_waitcnt lgkmcnt(6)\n\t"
    "v_cvt_pk_bf16_f32 v210, v224, v225\n\t"
    "v_cvt_pk_bf16_f32 v211, v226, v227\n\t"
    "v_mfma_f32_16x16x32_bf16 v[120:123], v[24:27], v[144:147], 0\n\t"
    "s_waitcnt lgkmcnt(5)\n\t"
    "v_exp_f32 v116, v116\n\t"
    "v_mfma_f32_16x16x32_bf16 v[120:123], v[28:31], v[160:163], v[120:123]\n\t"
    "s_waitcnt lgkmcnt(4)\n\t"
    "v_exp_f32 v117, v117\n\t"
    "v_mfma_f32_16x16x32_bf16 v[120:123], v[32:35], v[148:151], v[120:123]\n\t"
    "s_waitcnt lgkmcnt(3)\n\t"
    "v_exp_f32 v118, v118\n\t"
    "v_mfma_f32_16x16x32_bf16 v[120:123], v[36:39], v[164:167], v[120:123]\n\t"
    "s_waitcnt lgkmcnt(2)\n\t"
    "v_exp_f32 v119, v119\n\t"
    "v_mfma_f32_16x16x32_bf16 v[120:123], v[40:43], v[152:155], v[120:123]\n\t"
    "s_waitcnt lgkmcnt(1)\n\t"
    "v_fma_f32 v240, v240, s62, v129\n\t"
    "v_fma_f32 v241, v241, s62, v129\n\t"
    "v_mfma_f32_16x16x32_bf16 v[120:123], v[44:47], v[168:171], v[120:123]\n\t"
    "v_fma_f32 v242, v242, s62, v129\n\t"
    "v_fma_f32 v243, v243, s62, v129\n\t"
    "v_mfma_f32_16x16x32_bf16 v[156:159], a[236:239], v[168:171], 0\n\t"
    "v_cvt_pk_bf16_f32 v116, v116, v117\n\t"
    "v_cvt_pk_bf16_f32 v117, v118, v119\n\t"
    "v_mfma_f32_16x16x32_bf16 v[156:159], a[232:235], v[152:155], v[156:159]\n\t"
    "v_exp_f32 v240, v240\n\t"
    "v_mfma_f32_16x16x32_bf16 v[156:159], a[228:231], v[164:167], v[156:159]\n\t"
    "v_exp_f32 v241, v241\n\t"
    "v_mfma_f32_16x16x32_bf16 v[156:159], a[224:227], v[148:151], v[156:159]\n\t"
    "v_exp_f32 v242, v242\n\t"
    "v_mfma_f32_16x16x32_bf16 v[156:159], a[220:223], v[160:163], v[156:159]\n\t"
    "v_exp_f32 v243, v243\n\t"
    "v_mfma_f32_16x16x32_bf16 v[156:159], a[216:219], v[144:147], v[156:159]\n\t"
    "v_cvt_pk_bf16_f32 v118, v240, v241\n\t"
    "v_cvt_pk_bf16_f32 v119, v242, v243\n\t"
    "v_mfma_f32_16x16x32_bf16 v[252:255], a[240:243], v[144:147], 0\n\t"
    "ds_read_b128 v[144:147], v3 offset:18432\n\t"
    "v_cvt_pk_bf16_f32 v120, v120, v121\n\t"
    "v_mfma_f32_16x16x32_bf16 v[252:255], a[244:247], v[160:163], v[252:255]\n\t"
    "ds_read_b128 v[160:163], v3 offset:19456\n\t"
    "v_cvt_pk_bf16_f32 v121, v122, v123\n\t"
    "v_mfma_f32_16x16x32_bf16 v[252:255], a[248:251], v[148:151], v[252:255]\n\t"
    "ds_read_b128 v[148:151], v3 offset:20480\n\t"
    "v_cvt_pk_bf16_f32 v156, v156, v157\n\t"
    "v_mfma_f32_16x16x32_bf16 v[252:255], a[252:255], v[164:167], v[252:255]\n\t"
    "ds_read_b128 v[164:167], v3 offset:21504\n\t"
    "v_cvt_pk_bf16_f32 v157, v158, v159\n\t"
    "v_mfma_f32_16x16x32_bf16 v[252:255], v[16:19], v[152:155], v[252:255]\n\t"
    "ds_read_b128 v[152:155], v3 offset:22528\n\t"
    "v_fma_f32 v212, v212, s62, v132\n\t"
    "v_mfma_f32_16x16x32_bf16 v[252:255], v[20:23], v[168:171], v[252:255]\n\t"
    "ds_read_b128 v[168:171], v3 offset:23552\n\t"
    "v_fma_f32 v213, v213, s62, v132\n\t"
    "v_mfma_f32_16x16x32_bf16 v[224:227], v[12:15], v[208:211], 0\n\t"
    "ds_write_b64 v7, v[120:121] offset:4096\n\t"
    "v_mfma_f32_16x16x32_bf16 v[224:227], v[12:15], v[116:119], v[224:227]\n\t"
    "v_exp_f32 v212, v212\n\t"
    "v_mfma_f32_16x16x32_bf16 v[240:243], v[176:179], v[208:211], 0\n\t"
    "v_exp_f32 v213, v213\n\t"
    "v_mfma_f32_16x16x32_bf16 v[240:243], v[180:183], v[116:119], v[240:243]\n\t"
    "v_cvt_pk_bf16_f32 v158, v252, v253\n\t"
    "v_cvt_pk_bf16_f32 v159, v254, v255\n\t"
    "v_mfma_f32_16x16x32_bf16 v[204:207], v[184:187], v[208:211], 0\n\t"
    "ds_write_b128 v6, v[156:159] offset:4096\n\t"
    "v_mfma_f32_16x16x32_bf16 v[204:207], v[188:191], v[116:119], v[204:207]\n\t"
    "v_rcp_f32 v140, v224\n\t"
    "v_mfma_f32_16x16x32_bf16 v[112:115], v[192:195], v[208:211], 0\n\t"
    "s_waitcnt vmcnt(0) lgkmcnt(2)\n\t"
    "s_barrier\n\t"
    "ds_read_b128 v[224:227], v1 offset:16384\n\t"
    "v_fma_f32 v214, v214, s62, v132\n\t"
    "v_mfma_f32_16x16x32_bf16 v[112:115], v[196:199], v[116:119], v[112:115]\n\t"
    "s_add_u32 s60, s60, 1\n\t"
    "v_fma_f32 v215, v215, s62, v132\n\t"
    "v_fma_f32 v228, v228, s62, v132\n\t"
    "v_mfma_f32_16x16x32_bf16 v[120:123], v[24:27], v[144:147], 0\n\t"
    "s_cmp_lt_u32 s60, 9\n\t"
    "v_exp_f32 v214, v214\n\t"
    "v_mfma_f32_16x16x32_bf16 v[120:123], v[28:31], v[160:163], v[120:123]\n\t"
    "v_exp_f32 v215, v215\n\t"
    "v_mfma_f32_16x16x32_bf16 v[120:123], v[32:35], v[148:151], v[120:123]\n\t"
    "v_fma_f32 v229, v229, s62, v132\n\t"
    "v_fma_f32 v230, v230, s62, v132\n\t"
    "v_mfma_f32_16x16x32_bf16 v[120:123], v[36:39], v[164:167], v[120:123]\n\t"
    "v_fma_f32 v231, v231, s62, v132\n\t"
    "v_cvt_pk_bf16_f32 v212, v212, v213\n\t"
    "v_mfma_f32_16x16x32_bf16 v[120:123], v[40:43], v[152:155], v[120:123]\n\t"
    "v_exp_f32 v228, v228\n\t"
    "v_mfma_f32_16x16x32_bf16 v[120:123], v[44:47], v[168:171], v[120:123]\n\t"
    "v_exp_f32 v229, v229\n\t"
    "v_mfma_f32_16x16x32_bf16 v[156:159], a[236:239], v[168:171], 0\n\t"
    "v_exp_f32 v230, v230\n\t"
    "v_mfma_f32_16x16x32_bf16 v[156:159], a[232:235], v[152:155], v[156:159]\n\t"
    "v_exp_f32 v231, v231\n\t"
    "v_mfma_f32_16x16x32_bf16 v[156:159], a[228:231], v[164:167], v[156:159]\n\t"
    "v_cvt_pk_bf16_f32 v213, v214, v215\n\t"
    "v_fma_f32 v124, v124, s62, v132\n\t"
    "v_mfma_f32_16x16x32_bf16 v[156:159], a[224:227], v[148:151], v[156:159]\n\t"
    "v_fma_f32 v125, v125, s62, v132\n\t"
    "v_cvt_pk_bf16_f32 v214, v228, v229\n\t"
    "v_mfma_f32_16x16x32_bf16 v[156:159], a[220:223], v[160:163], v[156:159]\n\t"
    "v_cvt_pk_bf16_f32 v215, v230, v231\n\t"
    "v_fma_f32 v126, v126, s62, v132\n\t"
    "v_mfma_f32_16x16x32_bf16 v[156:159], a[216:219], v[144:147], v[156:159]\n\t"
    "v_exp_f32 v124, v124\n\t"
    "v_mfma_f32_16x16x32_bf16 v[252:255], a[240:243], v[144:147], 0\n\t"
    "v_exp_f32 v125, v125\n\t"
    "v_mfma_f32_16x16x32_bf16 v[252:255], a[244:247], v[160:163], v[252:255]\n\t"
    "v_fma_f32 v127, v127, s62, v132\n\t"
    "v_fma_f32 v244, v244, s62, v132\n\t"
    "v_mfma_f32_16x16x32_bf16 v[252:255], a[248:251], v[148:151], v[252:255]\n\t"
    "ds_read_b128 v[148:151], v0 offset:0\n\t"
    "v_cvt_pk_bf16_f32 v156, v156, v157\n\t"
    "v_mfma_f32_16x16x32_bf16 v[252:255], a[252:255], v[164:167], v[252:255]\n\t"
    "ds_read_b128 v[164:167], v1 offset:8192\n\t"
    "v_cvt_pk_bf16_f32 v157, v158, v159\n\t"
    "v_mfma_f32_16x16x32_bf16 v[252:255], v[16:19], v[152:155], v[252:255]\n\t"
    "ds_read_b128 v[152:155], v1 offset:0\n\t"
    "v_fma_f32 v245, v245, s62, v132\n\t"
    "v_mfma_f32_16x16x32_bf16 v[252:255], v[20:23], v[168:171], v[252:255]\n\t"
    "ds_read_b128 v[168:171], v0 offset:16384\n\t"
    "v_fma_f32 v246, v246, s62, v132\n\t"
    "v_mfma_f32_16x16x32_bf16 v[228:231], v[12:15], v[212:215], 0\n\t"
    "v_exp_f32 v126, v126\n\t"
    "v_exp_f32 v127, v127\n\t"
    "v_fma_f32 v247, v247, s62, v132\n\t"
    "v_cvt_pk_bf16_f32 v124, v124, v125\n\t"
    "v_exp_f32 v244, v244\n\t"
    "v_exp_f32 v245, v245\n\t"
    "v_exp_f32 v246, v246\n\t"
    "v_exp_f32 v247, v247\n\t"
    "v_cvt_pk_bf16_f32 v125, v126, v127\n\t"
    "v_cvt_pk_bf16_f32 v126, v244, v245\n\t"
    "v_cvt_pk_bf16_f32 v127, v246, v247\n\t"
    "v_cvt_pk_bf16_f32 v158, v252, v253\n\t"
    "v_cvt_pk_bf16_f32 v159, v254, v255\n\t"
    "v_mfma_f32_16x16x32_bf16 v[228:231], v[12:15], v[124:127], v[228:231]\n\t"
    "ds_write_b128 v6, v[156:159] offset:6144\n\t"
    "v_mfma_f32_16x16x32_bf16 v[244:247], v[176:179], v[212:215], 0\n\t"
    "ds_read_b128 v[156:159], v0 offset:8192\n\t"
    "v_cvt_pk_bf16_f32 v120, v120, v121\n\t"
    "v_mfma_f32_16x16x32_bf16 v[244:247], v[180:183], v[124:127], v[244:247]\n\t"
    "v_cvt_pk_bf16_f32 v121, v122, v123\n\t"
    "v_xor_b32 v8, 0x38000, v8\n\t"
    "v_mfma_f32_16x16x32_bf16 v[208:211], v[184:187], v[212:215], 0\n\t"
    "v_rcp_f32 v141, v228\n\t"
    "v_mfma_f32_16x16x32_bf16 v[208:211], v[188:191], v[124:127], v[208:211]\n\t"
    "ds_write_b64 v7, v[120:121] offset:6144\n\t"
    "v_mfma_f32_16x16x32_bf16 v[116:119], v[192:195], v[212:215], 0\n\t"
    "v_xor_b32 v6, 0x38000, v6\n\t"
    "v_xor_b32 v7, 0x38000, v7\n\t"
    "v_mfma_f32_16x16x32_bf16 v[116:119], v[196:199], v[124:127], v[116:119]\n\t"
    "s_cbranch_scc1 LOOP_%=\n\t"
    "TAIL_%=:\n\t"
    "s_mov_b32 s46, 56623104\n\t"
    "v_mul_f32 v232, v136, v232\n\t"
    "v_mul_f32 v233, v136, v233\n\t"
    "v_mul_f32 v234, v136, v234\n\t"
    "v_mul_f32 v235, v136, v235\n\t"
    "v_cvt_pk_bf16_f32 v232, v232, v233\n\t"
    "v_mul_f32 v216, v136, v216\n\t"
    "v_mul_f32 v217, v136, v217\n\t"
    "v_cvt_pk_bf16_f32 v233, v234, v235\n\t"
    "v_mul_f32 v172, v136, v172\n\t"
    "v_mul_f32 v173, v136, v173\n\t"
    "v_mul_f32 v174, v136, v174\n\t"
    "v_mul_f32 v175, v136, v175\n\t"
    "v_mul_f32 v218, v136, v218\n\t"
    "v_mul_f32 v219, v136, v219\n\t"
    "v_cvt_pk_bf16_f32 v216, v216, v217\n\t"
    "v_cvt_pk_bf16_f32 v234, v172, v173\n\t"
    "v_cvt_pk_bf16_f32 v235, v174, v175\n\t"
    "v_cvt_pk_bf16_f32 v217, v218, v219\n\t"
    "ds_write_b128 v4, v[232:235] offset:0\n\t"
    "v_mul_f32 v236, v133, v236\n\t"
    "v_mul_f32 v237, v133, v237\n\t"
    "v_mul_f32 v238, v133, v238\n\t"
    "v_mul_f32 v239, v133, v239\n\t"
    "v_cvt_pk_bf16_f32 v236, v236, v237\n\t"
    "v_mul_f32 v248, v133, v248\n\t"
    "v_mul_f32 v249, v133, v249\n\t"
    "v_cvt_pk_bf16_f32 v237, v238, v239\n\t"
    "v_mul_f32 v200, v133, v200\n\t"
    "ds_write_b64 v5, v[216:217] offset:0\n\t"
    "s_waitcnt lgkmcnt(0)\n\t"
    "s_barrier\n\t"
    "ds_read_b128 v[144:147], v3 offset:0\n\t"
    "ds_read_b128 v[160:163], v3 offset:1024\n\t"
    "ds_read_b128 v[148:151], v3 offset:2048\n\t"
    "ds_read_b128 v[164:167], v3 offset:3072\n\t"
    "ds_read_b128 v[152:155], v3 offset:4096\n\t"
    "ds_read_b128 v[168:171], v3 offset:5120\n\t"
    "v_mul_f32 v201, v133, v201\n\t"
    "v_mul_f32 v202, v133, v202\n\t"
    "v_mul_f32 v203, v133, v203\n\t"
    "v_mul_f32 v250, v133, v250\n\t"
    "v_mul_f32 v251, v133, v251\n\t"
    "v_cvt_pk_bf16_f32 v248, v248, v249\n\t"
    "v_cvt_pk_bf16_f32 v238, v200, v201\n\t"
    "v_cvt_pk_bf16_f32 v239, v202, v203\n\t"
    "v_cvt_pk_bf16_f32 v249, v250, v251\n\t"
    "ds_write_b128 v4, v[236:239] offset:6144\n\t"
    "v_mul_f32 v240, v140, v240\n\t"
    "v_mul_f32 v241, v140, v241\n\t"
    "v_mul_f32 v242, v140, v242\n\t"
    "v_mul_f32 v243, v140, v243\n\t"
    "v_cvt_pk_bf16_f32 v240, v240, v241\n\t"
    "s_waitcnt lgkmcnt(6)\n\t"
    "v_mfma_f32_16x16x32_bf16 v[120:123], v[24:27], v[144:147], 0\n\t"
    "s_waitcnt lgkmcnt(5)\n\t"
    "v_mul_f32 v112, v140, v112\n\t"
    "v_mul_f32 v113, v140, v113\n\t"
    "v_mfma_f32_16x16x32_bf16 v[120:123], v[28:31], v[160:163], v[120:123]\n\t"
    "s_waitcnt lgkmcnt(4)\n\t"
    "ds_write_b64 v5, v[248:249] offset:6144\n\t"
    "v_mfma_f32_16x16x32_bf16 v[120:123], v[32:35], v[148:151], v[120:123]\n\t"
    "s_waitcnt lgkmcnt(4)\n\t"
    "v_cvt_pk_bf16_f32 v241, v242, v243\n\t"
    "v_mul_f32 v204, v140, v204\n\t"
    "v_mfma_f32_16x16x32_bf16 v[120:123], v[36:39], v[164:167], v[120:123]\n\t"
    "s_waitcnt lgkmcnt(3)\n\t"
    "v_mul_f32 v205, v140, v205\n\t"
    "v_mul_f32 v206, v140, v206\n\t"
    "v_mfma_f32_16x16x32_bf16 v[120:123], v[40:43], v[152:155], v[120:123]\n\t"
    "s_waitcnt lgkmcnt(2)\n\t"
    "v_mul_f32 v207, v140, v207\n\t"
    "v_mul_f32 v114, v140, v114\n\t"
    "v_mfma_f32_16x16x32_bf16 v[120:123], v[44:47], v[168:171], v[120:123]\n\t"
    "v_mul_f32 v115, v140, v115\n\t"
    "v_cvt_pk_bf16_f32 v112, v112, v113\n\t"
    "v_mfma_f32_16x16x32_bf16 v[220:223], a[236:239], v[168:171], 0\n\t"
    "v_cvt_pk_bf16_f32 v242, v204, v205\n\t"
    "v_cvt_pk_bf16_f32 v243, v206, v207\n\t"
    "v_mfma_f32_16x16x32_bf16 v[220:223], a[232:235], v[152:155], v[220:223]\n\t"
    "v_cvt_pk_bf16_f32 v113, v114, v115\n\t"
    "v_mul_f32 v244, v141, v244\n\t"
    "v_mfma_f32_16x16x32_bf16 v[220:223], a[228:231], v[164:167], v[220:223]\n\t"
    "s_waitcnt lgkmcnt(0)\n\t"
    "s_barrier\n\t"
    "ds_read_b128 v[156:159], v3 offset:6144\n\t"
    "ds_read_b128 v[224:227], v3 offset:7168\n\t"
    "v_mfma_f32_16x16x32_bf16 v[220:223], a[224:227], v[148:151], v[220:223]\n\t"
    "ds_read_b128 v[228:231], v3 offset:8192\n\t"
    "ds_read_b128 v[124:127], v3 offset:9216\n\t"
    "v_mfma_f32_16x16x32_bf16 v[220:223], a[220:223], v[160:163], v[220:223]\n\t"
    "ds_read_b128 v[252:255], v3 offset:10240\n\t"
    "ds_read_b128 v[172:175], v3 offset:11264\n\t"
    "v_mfma_f32_16x16x32_bf16 v[220:223], a[216:219], v[144:147], v[220:223]\n\t"
    "ds_read_b128 v[176:179], v8 offset:0\n\t"
    "v_cvt_pk_bf16_f32 v120, v120, v121\n\t"
    "v_mfma_f32_16x16x32_bf16 v[212:215], a[240:243], v[144:147], 0\n\t"
    "ds_write_b128 v4, v[240:243] offset:12288\n\t"
    "v_mfma_f32_16x16x32_bf16 v[212:215], a[244:247], v[160:163], v[212:215]\n\t"
    "v_cvt_pk_bf16_f32 v121, v122, v123\n\t"
    "v_mul_f32 v245, v141, v245\n\t"
    "v_mfma_f32_16x16x32_bf16 v[212:215], a[248:251], v[148:151], v[212:215]\n\t"
    "v_mul_f32 v246, v141, v246\n\t"
    "v_mul_f32 v247, v141, v247\n\t"
    "v_mfma_f32_16x16x32_bf16 v[212:215], a[252:255], v[164:167], v[212:215]\n\t"
    "ds_read_b128 v[180:183], v8 offset:8192\n\t"
    "v_cvt_pk_bf16_f32 v244, v244, v245\n\t"
    "v_mfma_f32_16x16x32_bf16 v[212:215], v[16:19], v[152:155], v[212:215]\n\t"
    "ds_write_b64 v5, v[112:113] offset:12288\n\t"
    "v_mfma_f32_16x16x32_bf16 v[212:215], v[20:23], v[168:171], v[212:215]\n\t"
    "s_waitcnt lgkmcnt(3)\n\t"
    "buffer_store_dwordx4 v[176:179], v9, s[44:47], s52 offen offset:0 nt\n\t"
    "v_mfma_f32_16x16x32_bf16 v[232:235], v[24:27], v[156:159], 0\n\t"
    "ds_write_b64 v7, v[120:121] offset:0\n\t"
    "v_mfma_f32_16x16x32_bf16 v[232:235], v[28:31], v[224:227], v[232:235]\n\t"
    "ds_read_b128 v[184:187], v8 offset:16384\n\t"
    "v_mul_f32 v116, v141, v116\n\t"
    "v_mfma_f32_16x16x32_bf16 v[232:235], v[32:35], v[228:231], v[232:235]\n\t"
    "v_mul_f32 v117, v141, v117\n\t"
    "v_cvt_pk_bf16_f32 v245, v246, v247\n\t"
    "v_mfma_f32_16x16x32_bf16 v[232:235], v[36:39], v[124:127], v[232:235]\n\t"
    "s_waitcnt lgkmcnt(3)\n\t"
    "buffer_store_dwordx4 v[180:183], v9, s[44:47], s52 offen offset:128 nt\n\t"
    "v_mfma_f32_16x16x32_bf16 v[232:235], v[40:43], v[252:255], v[232:235]\n\t"
    "s_waitcnt lgkmcnt(2)\n\t"
    "s_barrier\n\t"
    "ds_read_b128 v[144:147], v3 offset:12288\n\t"
    "ds_read_b128 v[160:163], v3 offset:13312\n\t"
    "v_mfma_f32_16x16x32_bf16 v[232:235], v[44:47], v[172:175], v[232:235]\n\t"
    "ds_read_b128 v[188:191], v8 offset:4096\n\t"
    "ds_read_b128 v[148:151], v3 offset:14336\n\t"
    "v_mfma_f32_16x16x32_bf16 v[216:219], a[236:239], v[172:175], 0\n\t"
    "ds_read_b128 v[164:167], v3 offset:15360\n\t"
    "ds_read_b128 v[152:155], v3 offset:16384\n\t"
    "v_mfma_f32_16x16x32_bf16 v[216:219], a[232:235], v[252:255], v[216:219]\n\t"
    "s_waitcnt lgkmcnt(6)\n\t"
    "buffer_store_dwordx4 v[184:187], v9, s[44:47], s52 offen offset:256 nt\n\t"
    "v_mfma_f32_16x16x32_bf16 v[216:219], a[228:231], v[124:127], v[216:219]\n\t"
    "ds_read_b128 v[168:171], v3 offset:17408\n\t"
    "v_mul_f32 v208, v141, v208\n\t"
    "v_mfma_f32_16x16x32_bf16 v[216:219], a[224:227], v[228:231], v[216:219]\n\t"
    "ds_read_b128 v[192:195], v8 offset:12288\n\t"
    "v_mul_f32 v209, v141, v209\n\t"
    "v_mfma_f32_16x16x32_bf16 v[216:219], a[220:223], v[224:227], v[216:219]\n\t"
    "v_mul_f32 v210, v141, v210\n\t"
    "v_mul_f32 v211, v141, v211\n\t"
    "v_mfma_f32_16x16x32_bf16 v[216:219], a[216:219], v[156:159], v[216:219]\n\t"
    "s_waitcnt lgkmcnt(5)\n\t"
    "buffer_store_dwordx4 v[188:191], v9, s[44:47], s53 offen offset:0 nt\n\t"
    "v_mfma_f32_16x16x32_bf16 v[236:239], a[240:243], v[156:159], 0\n\t"
    "v_mul_f32 v118, v141, v118\n\t"
    "v_mul_f32 v119, v141, v119\n\t"
    "v_mfma_f32_16x16x32_bf16 v[236:239], a[244:247], v[224:227], v[236:239]\n\t"
    "ds_read_b128 v[196:199], v8 offset:20480\n\t"
    "v_cvt_pk_bf16_f32 v116, v116, v117\n\t"
    "v_mfma_f32_16x16x32_bf16 v[236:239], a[248:251], v[228:231], v[236:239]\n\t"
    "v_cvt_pk_bf16_f32 v246, v208, v209\n\t"
    "v_cvt_pk_bf16_f32 v247, v210, v211\n\t"
    "v_mfma_f32_16x16x32_bf16 v[236:239], a[252:255], v[124:127], v[236:239]\n\t"
    "s_waitcnt lgkmcnt(1)\n\t"
    "buffer_store_dwordx4 v[192:195], v9, s[44:47], s53 offen offset:128 nt\n\t"
    "v_mfma_f32_16x16x32_bf16 v[236:239], v[16:19], v[252:255], v[236:239]\n\t"
    "v_cvt_pk_bf16_f32 v117, v118, v119\n\t"
    "v_cvt_pk_bf16_f32 v220, v220, v221\n\t"
    "v_mfma_f32_16x16x32_bf16 v[236:239], v[20:23], v[172:175], v[236:239]\n\t"
    "ds_write_b128 v4, v[244:247] offset:18432\n\t"
    "v_mfma_f32_16x16x32_bf16 v[120:123], v[24:27], v[144:147], 0\n\t"
    "v_cvt_pk_bf16_f32 v221, v222, v223\n\t"
    "v_cvt_pk_bf16_f32 v222, v212, v213\n\t"
    "v_mfma_f32_16x16x32_bf16 v[120:123], v[28:31], v[160:163], v[120:123]\n\t"
    "s_waitcnt lgkmcnt(1)\n\t"
    "buffer_store_dwordx4 v[196:199], v9, s[44:47], s53 offen offset:256 nt\n\t"
    "v_mfma_f32_16x16x32_bf16 v[120:123], v[32:35], v[148:151], v[120:123]\n\t"
    "ds_write_b64 v5, v[116:117] offset:18432\n\t"
    "v_mfma_f32_16x16x32_bf16 v[120:123], v[36:39], v[164:167], v[120:123]\n\t"
    "v_cvt_pk_bf16_f32 v223, v214, v215\n\t"
    "v_cvt_pk_bf16_f32 v232, v232, v233\n\t"
    "v_mfma_f32_16x16x32_bf16 v[120:123], v[40:43], v[152:155], v[120:123]\n\t"
    "ds_write_b128 v6, v[220:223] offset:0\n\t"
    "v_mfma_f32_16x16x32_bf16 v[120:123], v[44:47], v[168:171], v[120:123]\n\t"
    "v_cvt_pk_bf16_f32 v233, v234, v235\n\t"
    "v_cvt_pk_bf16_f32 v216, v216, v217\n\t"
    "v_mfma_f32_16x16x32_bf16 v[220:223], a[236:239], v[168:171], 0\n\t"
    "v_cvt_pk_bf16_f32 v217, v218, v219\n\t"
    "v_cvt_pk_bf16_f32 v218, v236, v237\n\t"
    "v_mfma_f32_16x16x32_bf16 v[220:223], a[232:235], v[152:155], v[220:223]\n\t"
    "ds_write_b64 v7, v[232:233] offset:2048\n\t"
    "v_mfma_f32_16x16x32_bf16 v[220:223], a[228:231], v[164:167], v[220:223]\n\t"
    "s_waitcnt lgkmcnt(2)\n\t"
    "s_barrier\n\t"
    "ds_read_b128 v[156:159], v3 offset:18432\n\t"
    "ds_read_b128 v[224:227], v3 offset:19456\n\t"
    "v_mfma_f32_16x16x32_bf16 v[220:223], a[224:227], v[148:151], v[220:223]\n\t"
    "ds_read_b128 v[228:231], v3 offset:20480\n\t"
    "ds_read_b128 v[124:127], v3 offset:21504\n\t"
    "v_mfma_f32_16x16x32_bf16 v[220:223], a[220:223], v[160:163], v[220:223]\n\t"
    "ds_read_b128 v[252:255], v3 offset:22528\n\t"
    "ds_read_b128 v[172:175], v3 offset:23552\n\t"
    "v_mfma_f32_16x16x32_bf16 v[220:223], a[216:219], v[144:147], v[220:223]\n\t"
    "v_cvt_pk_bf16_f32 v219, v238, v239\n\t"
    "v_cvt_pk_bf16_f32 v120, v120, v121\n\t"
    "v_mfma_f32_16x16x32_bf16 v[212:215], a[240:243], v[144:147], 0\n\t"
    "ds_write_b128 v6, v[216:219] offset:2048\n\t"
    "v_mfma_f32_16x16x32_bf16 v[212:215], a[244:247], v[160:163], v[212:215]\n\t"
    "v_cvt_pk_bf16_f32 v121, v122, v123\n\t"
    "v_xor_b32 v8, 0x38000, v8\n\t"
    "v_mfma_f32_16x16x32_bf16 v[212:215], a[248:251], v[148:151], v[212:215]\n\t"
    "v_cvt_pk_bf16_f32 v220, v220, v221\n\t"
    "v_cvt_pk_bf16_f32 v221, v222, v223\n\t"
    "v_mfma_f32_16x16x32_bf16 v[212:215], a[252:255], v[164:167], v[212:215]\n\t"
    "ds_write_b64 v7, v[120:121] offset:4096\n\t"
    "v_mfma_f32_16x16x32_bf16 v[212:215], v[16:19], v[152:155], v[212:215]\n\t"
    "v_mfma_f32_16x16x32_bf16 v[212:215], v[20:23], v[168:171], v[212:215]\n\t"
    "s_waitcnt lgkmcnt(7)\n\t"
    "v_mfma_f32_16x16x32_bf16 v[232:235], v[24:27], v[156:159], 0\n\t"
    "s_waitcnt lgkmcnt(6)\n\t"
    "v_mfma_f32_16x16x32_bf16 v[232:235], v[28:31], v[224:227], v[232:235]\n\t"
    "s_waitcnt lgkmcnt(1)\n\t"
    "s_barrier\n\t"
    "ds_read_b128 v[176:179], v8 offset:0\n\t"
    "ds_read_b128 v[180:183], v8 offset:8192\n\t"
    "v_mfma_f32_16x16x32_bf16 v[232:235], v[32:35], v[228:231], v[232:235]\n\t"
    "ds_read_b128 v[184:187], v8 offset:16384\n\t"
    "v_mfma_f32_16x16x32_bf16 v[232:235], v[36:39], v[124:127], v[232:235]\n\t"
    "v_cvt_pk_bf16_f32 v222, v212, v213\n\t"
    "v_cvt_pk_bf16_f32 v223, v214, v215\n\t"
    "v_mfma_f32_16x16x32_bf16 v[232:235], v[40:43], v[252:255], v[232:235]\n\t"
    "ds_write_b128 v6, v[220:223] offset:4096\n\t"
    "v_mfma_f32_16x16x32_bf16 v[232:235], v[44:47], v[172:175], v[232:235]\n\t"
    "v_mfma_f32_16x16x32_bf16 v[216:219], a[236:239], v[172:175], 0\n\t"
    "v_mfma_f32_16x16x32_bf16 v[216:219], a[232:235], v[252:255], v[216:219]\n\t"
    "v_mfma_f32_16x16x32_bf16 v[216:219], a[228:231], v[124:127], v[216:219]\n\t"
    "s_waitcnt lgkmcnt(3)\n\t"
    "buffer_store_dwordx4 v[176:179], v9, s[44:47], s50 offen offset:0 nt\n\t"
    "v_mfma_f32_16x16x32_bf16 v[216:219], a[224:227], v[228:231], v[216:219]\n\t"
    "v_mfma_f32_16x16x32_bf16 v[216:219], a[220:223], v[224:227], v[216:219]\n\t"
    "v_mfma_f32_16x16x32_bf16 v[216:219], a[216:219], v[156:159], v[216:219]\n\t"
    "s_waitcnt lgkmcnt(2)\n\t"
    "buffer_store_dwordx4 v[180:183], v9, s[44:47], s50 offen offset:128 nt\n\t"
    "v_mfma_f32_16x16x32_bf16 v[236:239], a[240:243], v[156:159], 0\n\t"
    "v_cvt_pk_bf16_f32 v232, v232, v233\n\t"
    "v_cvt_pk_bf16_f32 v233, v234, v235\n\t"
    "v_mfma_f32_16x16x32_bf16 v[236:239], a[244:247], v[224:227], v[236:239]\n\t"
    "ds_write_b64 v7, v[232:233] offset:6144\n\t"
    "v_mfma_f32_16x16x32_bf16 v[236:239], a[248:251], v[228:231], v[236:239]\n\t"
    "s_waitcnt lgkmcnt(2)\n\t"
    "buffer_store_dwordx4 v[184:187], v9, s[44:47], s50 offen offset:256 nt\n\t"
    "v_mfma_f32_16x16x32_bf16 v[236:239], a[252:255], v[124:127], v[236:239]\n\t"
    "v_cvt_pk_bf16_f32 v216, v216, v217\n\t"
    "v_cvt_pk_bf16_f32 v217, v218, v219\n\t"
    "v_mfma_f32_16x16x32_bf16 v[236:239], v[16:19], v[252:255], v[236:239]\n\t"
    "v_mfma_f32_16x16x32_bf16 v[236:239], v[20:23], v[172:175], v[236:239]\n\t"
    "s_nop 7\n\t"
    "v_cvt_pk_bf16_f32 v218, v236, v237\n\t"
    "v_cvt_pk_bf16_f32 v219, v238, v239\n\t"
    "ds_write_b128 v6, v[216:219] offset:6144\n\t"
    "s_waitcnt lgkmcnt(0)\n\t"
    "s_barrier\n\t"
    "ds_read_b128 v[188:191], v8 offset:4096\n\t"
    "ds_read_b128 v[192:195], v8 offset:12288\n\t"
    "ds_read_b128 v[196:199], v8 offset:20480\n\t"
    "s_waitcnt lgkmcnt(2)\n\t"
    "buffer_store_dwordx4 v[188:191], v9, s[44:47], s51 offen offset:0 nt\n\t"
    "s_waitcnt lgkmcnt(1)\n\t"
    "buffer_store_dwordx4 v[192:195], v9, s[44:47], s51 offen offset:128 nt\n\t"
    "s_waitcnt lgkmcnt(0)\n\t"
    "buffer_store_dwordx4 v[196:199], v9, s[44:47], s51 offen offset:256 nt\n\t"
    "END_%=:\n\t"
    : "+{v0}"(vXE), "+{v1}"(vXO), "+{v2}"(vM), "+{v3}"(vOR), "+{v4}"(vOW), "+{v5}"(vOW2),
      "+{v6}"(vSW), "+{v7}"(vSW2), "+{v8}"(vSR), "+{v9}"(vG), "+{v10}"(vGM), "+{v11}"(vL16),
      "+{v130}"(vI0), "+{v131}"(vI1), "+{v44}"(vTo0), "+{v45}"(vTo1), "+{v46}"(vTo2), "+{v47}"(vTo3),
      "+{v251}"(vBW), "+{v255}"(vBR), "+{v24}"(vT0), "+{v252}"(vU0), "+{v224}"(vD0), "+{v40}"(vE0),
      "+{v25}"(vT1), "+{v253}"(vU1), "+{v225}"(vD1), "+{v41}"(vE1), "+{v26}"(vT2), "+{v254}"(vU2),
      "+{v226}"(vD2), "+{v42}"(vE2), "+{v14}"(vK0), "+{v15}"(vK1), "+{v[112:115]}"(vJ0), "+{v[116:119]}"(vJ1),
      "+{v[120:123]}"(vJ2), "+{v[124:127]}"(vJ3), "+{s58}"(sWH), "+{s59}"(sWW), "+{s60}"(sN), "+{s61}"(sXMW),
      "+{s62}"(sL2E), "+{s65}"(sYO), "+{s66}"(sXO), "+{s67}"(sSC), "+{s72}"(sWIN), "+{s73}"(sW),
      "+{s48}"(sA0), "+{s49}"(sA1), "+{s[36:39]}"(srdX), "+{s[40:43]}"(srdM), "+{s[44:47]}"(srdO), "+{s[76:79]}"(srdW),
      "+{s[28:31]}"(srdWo)
    :
    : "v12", "v13", "v16", "v17", "v18", "v19", "v20", "v21", "v22", "v23", "v27", "v28", "v29", "v30", "v31", "v32", "v33", "v34", "v35", "v36", "v37", "v38", "v39", "v43", "v48", "v49", "v50", "v51", "v52", "v53", "v54", "v55", "v56", "v57", "v58", "v59", "v60", "v61", "v62", "v63", "v64", "v65", "v66", "v67", "v68", "v69", "v70", "v71", "v72", "v73", "v74", "v75", "v76", "v77", "v78", "v79", "v80", "v81", "v82", "v83", "v84", "v85", "v86", "v87", "v88", "v89", "v90", "v91", "v92", "v93", "v94", "v95", "v96", "v97", "v98", "v99", "v100", "v101", "v102", "v103", "v104", "v105", "v106", "v107", "v108", "v109", "v110", "v111", "v128", "v129", "v132", "v133", "v134", "v135", "v136", "v137", "v138", "v139", "v140", "v141", "v142", "v143", "v144", "v145", "v146", "v147", "v148", "v149", "v150", "v151", "v152", "v153", "v154", "v155", "v156", "v157", "v158", "v159", "v160", "v161", "v162", "v163", "v164", "v165", "v166", "v167", "v168", "v169", "v170", "v171", "v172", "v173", "v174", "v175", "v176", "v177", "v178", "v179", "v180", "v181", "v182", "v183", "v184", "v185", "v186", "v187", "v188", "v189", "v190", "v191", "v192", "v193", "v194", "v195", "v196", "v197", "v198", "v199", "v200", "v201", "v202", "v203", "v204", "v205", "v206", "v207", "v208", "v209", "v210", "v211", "v212", "v213", "v214", "v215", "v216", "v217", "v218", "v219", "v220", "v221", "v222", "v223", "v227", "v228", "v229", "v230", "v231", "v232", "v233", "v234", "v235", "v236", "v237", "v238", "v239", "v240", "v241", "v242", "v243", "v244", "v245", "v246", "v247", "v248", "v249", "v250", "a0", "a1", "a2", "a3", "a4", "a5", "a6", "a7", "a8", "a9", "a10", "a11", "a12", "a13", "a14", "a15", "a16", "a17", "a18", "a19", "a20", "a21", "a22", "a23", "a24", "a25", "a26", "a27", "a28", "a29", "a30", "a31", "a32", "a33", "a34", "a35", "a36", "a37", "a38", "a39", "a40", "a41", "a42", "a43", "a44", "a45", "a46", "a47", "a48", "a49", "a50", "a51", "a52", "a53", "a54", "a55", "a56", "a57", "a58", "a59", "a60", "a61", "a62", "a63", "a64", "a65", "a66", "a67", "a68", "a69", "a70", "a71", "a72", "a73", "a74", "a75", "a76", "a77", "a78", "a79", "a80", "a81", "a82", "a83", "a84", "a85", "a86", "a87", "a88", "a89", "a90", "a91", "a92", "a93", "a94", "a95", "a96", "a97", "a98", "a99", "a100", "a101", "a102", "a103", "a104", "a105", "a106", "a107", "a108", "a109", "a110", "a111", "a112", "a113", "a114", "a115", "a116", "a117", "a118", "a119", "a120", "a121", "a122", "a123", "a124", "a125", "a126", "a127", "a128", "a129", "a130", "a131", "a132", "a133", "a134", "a135", "a136", "a137", "a138", "a139", "a140", "a141", "a142", "a143", "a144", "a145", "a146", "a147", "a148", "a149", "a150", "a151", "a152", "a153", "a154", "a155", "a156", "a157", "a158", "a159", "a160", "a161", "a162", "a163", "a164", "a165", "a166", "a167", "a168", "a169", "a170", "a171", "a172", "a173", "a174", "a175", "a176", "a177", "a178", "a179", "a180", "a181", "a182", "a183", "a184", "a185", "a186", "a187", "a188", "a189", "a190", "a191", "a192", "a193", "a194", "a195", "a196", "a197", "a198", "a199", "a200", "a201", "a202", "a203", "a204", "a205", "a206", "a207", "a208", "a209", "a210", "a211", "a212", "a213", "a214", "a215", "a216", "a217", "a218", "a219", "a220", "a221", "a222", "a223", "a224", "a225", "a226", "a227", "a228", "a229", "a230", "a231", "a232", "a233", "a234", "a235", "a236", "a237", "a238", "a239", "a240", "a241", "a242", "a243", "a244", "a245", "a246", "a247", "a248", "a249", "a250", "a251", "a252", "a253", "a254", "a255", "s16", "s17", "s18", "s19", "s20", "s21", "s22", "s23", "s24", "s25", "s26", "s27", "s50", "s51", "s52", "s53", "s54", "s55", "s56", "s57", "s63", "s64", "s68", "s69", "s70", "s71", "s74", "s75", "scc", "vcc", "memory");
}

extern "C" void kernel_launch(void* const* d_in, const int* in_sizes, int n_in,
                              void* d_out, int out_size, void* d_ws, size_t ws_size,
                              hipStream_t stream) {
  (void)hipFuncSetAttribute((const void*)swin_kernel, hipFuncAttributeMaxDynamicSharedMemorySize, LDS_BYTES);
  swin_kernel<<<256, 256, LDS_BYTES, stream>>>((const char*)d_in[0], (const char*)d_in[1], (const char*)d_in[2], (const char*)d_in[3],
                                               (const char*)d_in[4], (const char*)d_in[5], (char*)d_out);
}
```
